```python
import torch

def _softmax(x, axis=-1):
    return torch.softmax(x, dim=axis)
def _gelu(x):
    return torch.nn.functional.gelu(x, approximate="tanh")
def _silu(x):
    return torch.nn.functional.silu(x)
def _sigmoid(x):
    return torch.sigmoid(x)
def _layernorm(x, axis=-1, eps=1e-5):
    m = x.mean(dim=axis, keepdim=True)
    v = x.var(dim=axis, keepdim=True, unbiased=False)
    return (x - m) / torch.sqrt(v + eps)
def _scatter_add(tgt, idx, src, axis=0):
    return tgt.clone().index_add_(axis, idx, src)
def _segment_sum(src, seg, n):
    out = torch.zeros((n,) + src.shape[1:], dtype=src.dtype)
    return out.index_add_(0, seg, src)
def _pad(x, pw):
    flat = tuple(v for pair in reversed(pw) for v in pair)
    return torch.nn.functional.pad(x, flat)

kernel_name = 'interleave_layers__sliding_local_dilated__dense_activation_silu__prompt'

def setup_inputs():

    _w = (lambda a: (lambda t: t.to(torch.bfloat16) if t.is_floating_point() else t)(torch.as_tensor(a)))
    def _A_setup():
        B, S, D = 1, 2048, 576
        return {
            "x": _w((lambda *s: torch.randn(*s, dtype=torch.bfloat16) * 0.05)(B, S, D)),
            "wqkv": _w((lambda *s: torch.randn(*s, dtype=torch.bfloat16) * 0.05)(D, 3 * D)),
            "wo": _w((lambda *s: torch.randn(*s, dtype=torch.bfloat16) * 0.05)(D, D)),
        }
    _A_in = _A_setup()

    def _B_setup():
        B, S, D, F = 1, 2048, 576, 4608
        return {
            "x": _w((lambda *s: torch.randn(*s, dtype=torch.bfloat16) * 0.05)(B, S, D)),
            "w1": _w((lambda *s: torch.randn(*s, dtype=torch.bfloat16) * 0.8)(D, F) * (D ** -0.5)),
            "w2": _w((lambda *s: torch.randn(*s, dtype=torch.bfloat16) * 0.8)(F, D) * (F ** -0.5)),
        }
    _B_in = _B_setup()

    return {
        "A_x": _A_in["x"],
        "A_wqkv": _A_in["wqkv"],
        "A_wo": _A_in["wo"],
        "B_x": _B_in["x"],
        "B_w1": _B_in["w1"],
        "B_w2": _B_in["w2"],
    }

def reference(A_x, A_wqkv, A_wo, B_x, B_w1, B_w2):
    def _A_ref(x, wqkv, wo):
        B, S, D = x.shape
        H = 32; Dh = D // H
        W, dil = 8, 2
        qkv = (lambda x, *d: x.permute(*d))((x @ wqkv).reshape(B, S, 3, H, Dh), 2, 0, 3, 1, 4)
        q, k, v = qkv[0], qkv[1], qkv[2]
        scale = Dh ** -0.5
        pos = torch.arange(S)
        off = torch.arange(W) * dil
        idx = pos[:, None] - off[None, :]
        mask = idx >= 0
        idx_c = torch.where(mask, idx, 0).reshape(-1)
        kg = k[:, :, idx_c, :].reshape(B, H, S, W, Dh)
        vg = v[:, :, idx_c, :].reshape(B, H, S, W, Dh)
        logits = torch.einsum("bhsd,bhswd->bhsw", q, kg) * scale
        logits = torch.where(mask[None, None, :, :], logits, -1e9)
        attn = _softmax(logits, axis=-1)
        o = torch.einsum("bhsw,bhswd->bhsd", attn, vg)
        return (lambda x, *d: x.permute(*d))(o, 0, 2, 1, 3).reshape(B, S, D) @ wo

    def _B_ref(x, w1, w2):
        h = torch.einsum("bsd,df->bsf", x, w1)
        h = _silu(h)
        out = torch.einsum("bsf,fd->bsd", h, w2)
        return out

    x = A_x
    x = x + _A_ref(_layernorm(x, axis=-1), A_wqkv, A_wo)
    x = x + _B_ref(_layernorm(x, axis=-1), B_w1, B_w2)
    x = x + _A_ref(_layernorm(x, axis=-1), A_wqkv, A_wo)
    x = x + _B_ref(_layernorm(x, axis=-1), B_w1, B_w2)
    return x
```

```cpp
#include <hip/hip_runtime.h>
#include <hip/hip_bf16.h>
#include <type_traits>

namespace {
typedef unsigned short u16;
typedef unsigned u32;
typedef unsigned long long u64;
typedef __attribute__((ext_vector_type(2))) unsigned u32x2;
typedef __attribute__((ext_vector_type(4))) unsigned u32x4;
typedef __attribute__((ext_vector_type(2))) __bf16 bf16x2;
typedef __attribute__((ext_vector_type(8))) __bf16 bf16x8;
typedef __attribute__((ext_vector_type(2))) float f32x2;
typedef __attribute__((ext_vector_type(4))) float f32x4;
typedef __attribute__((ext_vector_type(16))) float f32x16;
typedef __amdgpu_buffer_rsrc_t rsrc_t;
#define LAS __attribute__((address_space(3)))
#define DEVI __device__ __forceinline__
#define PIN(v) asm volatile("" : "+v"(v))
#define OPQ(v) asm volatile("" : "+s"(v))
#define SB() __builtin_amdgcn_sched_barrier(0)
#define BAR() do { SB(); asm volatile("s_barrier" ::: "memory"); SB(); } while (0)
#define WAITV(n) asm volatile("s_waitcnt vmcnt(" #n ")" ::: "memory")
#define WAITL() asm volatile("s_waitcnt lgkmcnt(0)" ::: "memory")

constexpr int SC1 = 16;
constexpr u32 OOB = 0x7ff00000u;
constexpr u32 OFF_NONCE = 0, OFF_MAIL = 32768, OFF_WT = OFF_MAIL + 256 * 4096;
constexpr u32 WT_PER = 864 * 1024, WT_O = 126 * 1024, WT_F = 216 * 1024;
constexpr u32 OFF_CS = OFF_WT + 16 * WT_PER, CS_PER = 36 * 1152;
constexpr u32 XB_SZ = 2048 * 576 * 2, OFF_XB = OFF_CS + 16 * CS_PER;
constexpr u32 ST_SZ = 131072, OFF_ST = OFF_XB + 4 * XB_SZ;
constexpr u32 OFF_OB = OFF_ST + 4 * ST_SZ;
constexpr u32 HB_SZ = 2048 * 4608 * 2, OFF_HB = OFF_OB + 2 * XB_SZ;
constexpr u32 PB_SZ = 4 * XB_SZ, OFF_PB = OFF_HB + 2 * HB_SZ;
constexpr u32 OFF_PROF = OFF_PB + 2 * PB_SZ, WS_END = OFF_PROF + 256 * 1024;
constexpr int SLOT = 36864;
constexpr int L_KS = 129024, L_VS = L_KS + 9216, L_QS = L_VS + 9216, L_RS = L_QS + 8192, L_CS1 = L_RS + 3456, L_CSQ = L_CS1 + 1152;
constexpr int LDS_BYTES = 163840;
constexpr float LOG2E = 1.4426950408889634f;
constexpr u32 ONES = 0x3F803F80u;

DEVI u32 pk(float a, float b) { f32x2 v = {a, b}; return __builtin_bit_cast(u32, __builtin_convertvector(v, bf16x2)); }
DEVI float lo16(u32 u) { return __uint_as_float(u << 16); }
DEVI float hi16(u32 u) { return __uint_as_float(u & 0xffff0000u); }
DEVI float dot2(u32 a, u32 b, float c) { return __builtin_amdgcn_fdot2_f32_bf16(__builtin_bit_cast(bf16x2, a), __builtin_bit_cast(bf16x2, b), c, false); }
DEVI float x16sum(float v) { auto s = __builtin_amdgcn_permlane16_swap(__float_as_uint(v), __float_as_uint(v), false, false); return __uint_as_float(s[0]) + __uint_as_float(s[1]); }
DEVI float x32sum(float v) { auto s = __builtin_amdgcn_permlane32_swap(__float_as_uint(v), __float_as_uint(v), false, false); return __uint_as_float(s[0]) + __uint_as_float(s[1]); }
DEVI u32 plo(u32 a, u32 b) { return __builtin_amdgcn_perm(b, a, 0x05040100u); }
DEVI u32 phi(u32 a, u32 b) { return __builtin_amdgcn_perm(b, a, 0x07060302u); }
DEVI f32x4 mfma16(u32x4 a, u32x4 b, f32x4 c) { return __builtin_amdgcn_mfma_f32_16x16x32_bf16(__builtin_bit_cast(bf16x8, a), __builtin_bit_cast(bf16x8, b), c, 0, 0, 0); }
DEVI void mac16(f32x4& c, u32x4 a, u32x4 b, bool zero) {
  if (zero) asm volatile("v_mfma_f32_16x16x32_bf16 %0, %1, %2, 0" : "=&v"(c) : "v"(a), "v"(b));
  else asm volatile("v_mfma_f32_16x16x32_bf16 %0, %1, %2, %0" : "+v"(c) : "v"(a), "v"(b));
}
DEVI void mac32(f32x16& c, u32x4 a, u32x4 b) { asm volatile("v_mfma_f32_32x32x16_bf16 %0, %1, %2, %0" : "+v"(c) : "v"(a), "v"(b)); }
DEVI void mul32(f32x16& c, u32x4 a, u32x4 b) { asm volatile("v_mfma_f32_32x32x16_bf16 %0, %1, %2, 0" : "=&v"(c) : "v"(a), "v"(b)); }
#define MFMA_DONE() asm volatile("s_nop 15")
DEVI u32x4 ld128(rsrc_t r, u32 v, u32 s) { return __builtin_amdgcn_raw_buffer_load_b128(r, v, s, SC1); }
DEVI u32x2 ld64(rsrc_t r, u32 v, u32 s) { return __builtin_amdgcn_raw_buffer_load_b64(r, v, s, SC1); }
DEVI void st128(u32x4 d, rsrc_t r, u32 v, u32 s) { __builtin_amdgcn_raw_buffer_store_b128(d, r, v, s, SC1); asm volatile("s_nop 1" :: "v"(d) : "memory"); }
DEVI void st64(u32x2 d, rsrc_t r, u32 v, u32 s) { __builtin_amdgcn_raw_buffer_store_b64(d, r, v, s, SC1); }
DEVI u32x4 lds128(const LAS char* p) { return *(const LAS u32x4*)p; }
DEVI rsrc_t mkrs(const void* p, u32 n) { return __builtin_amdgcn_make_buffer_rsrc((void*)p, 0, n, 0x00020000); }
DEVI int kcol0(int ks, int q) { return ks < 16 ? 72 * (ks >> 1) + 16 * q + 8 * (ks & 1) : 72 * (4 * (ks - 16) + q) + 64; }
DEVI void fin_stats(float s, float q, float& mean, float& rstd) {
  mean = s * (1.f / 576.f);
  float var = fmaxf(q * (1.f / 576.f) - mean * mean, 0.f);
  rstd = __builtin_amdgcn_rsqf(var + 1e-5f);
}
DEVI void stats16(u32x2 a, u32x2 b, float& mean, float& rstd) {
  float s = __uint_as_float(a[0]) + __uint_as_float(b[0]), q = __uint_as_float(a[1]) + __uint_as_float(b[1]);
  fin_stats(x32sum(x16sum(s)), x32sum(x16sum(q)), mean, rstd);
}
DEVI u32x4 chunk(const f32x4* t, int ab) {
  return u32x4{pk(t[0][2 * ab], t[1][2 * ab]), pk(t[2][2 * ab], t[3][2 * ab]), pk(t[0][2 * ab + 1], t[1][2 * ab + 1]), pk(t[2][2 * ab + 1], t[3][2 * ab + 1])};
}
DEVI void unchunk_add(f32x4* t, u32x4 c, int ab) {
  t[0][2 * ab] += lo16(c[0]); t[1][2 * ab] += hi16(c[0]); t[2][2 * ab] += lo16(c[1]); t[3][2 * ab] += hi16(c[1]);
  t[0][2 * ab + 1] += lo16(c[2]); t[1][2 * ab + 1] += hi16(c[2]); t[2][2 * ab + 1] += lo16(c[3]); t[3][2 * ab + 1] += hi16(c[3]);
}
template <int PACE> DEVI void dma9(rsrc_t ws, char* smem, int lds, u32 voff, u32 soff, int n) {
#pragma unroll 1
  for (int k = 0; k < n; ++k) {
#pragma unroll
    for (int i = 0; i < 9; ++i) {
      LAS void* d = (LAS void*)(smem + lds + (i / 3) * 3072);
      const u32 so = soff + (i / 3) * 3072;
      if (i % 3 == 0) __builtin_amdgcn_raw_ptr_buffer_load_lds(ws, d, 16, voff, so, 0, SC1);
      if (i % 3 == 1) __builtin_amdgcn_raw_ptr_buffer_load_lds(ws, d, 16, voff, so, 1024, SC1);
      if (i % 3 == 2) __builtin_amdgcn_raw_ptr_buffer_load_lds(ws, d, 16, voff, so, 2048, SC1);
      if (PACE && (i & 1)) __builtin_amdgcn_s_sleep(PACE);
    }
    lds += 9216; soff += 9216;
  }
}
DEVI u32x4 ldfrag(rsrc_t r, u32 v, u32 s, int byte) { return ld128(r, v + (byte & 4095), s + (byte & ~4095)); }
DEVI void poll(rsrc_t ws, u32 voff, u32 soff, bool active, u32x2 tag) {
  for (int spin = 0; spin < (1 << 17); ++spin) {
    u32x2 v = ld64(ws, voff, soff);
    asm volatile("" ::: "memory");
    if (__all(!active || (v[0] == tag[0] && v[1] == tag[1]))) break;
  }
  asm volatile("" ::: "memory");
}
DEVI u32x2 next_tag(u32x2 n) {
  u64 v = (((u64)n[1] << 32) | n[0]) * 6364136223846793005ull + 1442695040888963407ull;
  return u32x2{(u32)v, (u32)(v >> 32)};
}

#define STAMP(i)

__global__ void __launch_bounds__(512, 2)
fused_fwd(const u16* __restrict__ xin, const u16* __restrict__ wqkv, const u16* __restrict__ wo, const u16* __restrict__ w1,
          const u16* __restrict__ w2, u16* __restrict__ out, char* wsp) {
  extern __shared__ __attribute__((aligned(16))) char smem[];
  const int b = blockIdx.x, xcd = b & 7, rr_ = b >> 3;
  const int m = (xcd >> 2) * 8 + (rr_ >> 2), qd = xcd & 3, r = rr_ & 3, s = 4 * qd + r, cu = m * 16 + s;
  const int th = qd & 1, ch = qd >> 1, c8 = 2 * r + ch;
  const int wave = __builtin_amdgcn_readfirstlane(threadIdx.x >> 6);
  int l, t16, g4; u32 l16; bool mact;
  auto relane = [&]() {
    u32 z = 0; OPQ(z);
    l = __builtin_amdgcn_mbcnt_hi(~0u, __builtin_amdgcn_mbcnt_lo(~0u, z)); t16 = l & 15; g4 = l >> 4; l16 = l * 16; mact = (g4 >> 1) == ch;
  };
  relane();
  const bool first = (m & 7) == 0;
  const rsrc_t ws = mkrs(wsp, WS_END);
  const u32 wt = OFF_WT + s * WT_PER;
  LAS char* const lds = (LAS char*)smem;

  auto attention = [&](int L) {
    const int n = 16 + 16 * wave + t16, head = g4 >> 1, half = g4 & 1;
    const LAS char* kp = lds + L_KS + g4 * 2304 + (n - 7) * 16;
    const LAS char* rp = lds + L_RS + (n - 7) * 8 + head * 4;
    const u32x4 qc = lds128(lds + L_QS + g4 * 2048 + (n - 16) * 16);
    u32 qr = *(const LAS u32*)(rp + 2 * 1152 + 56);
    qr = half ? 0u : qr;
    float sc[8];
#pragma unroll
    for (int w = 0; w < 8; ++w) {
      const u32x4 kc = lds128(kp + (7 - w) * 16);
      const u32 kr = *(const LAS u32*)(rp + (7 - w) * 8);
      sc[w] = dot2(kc[3], qc[3], dot2(kc[2], qc[2], dot2(kc[1], qc[1], dot2(kc[0], qc[0], dot2(kr, qr, 0.f)))));
    }
    float p[4];
#pragma unroll
    for (int w = 0; w < 4; ++w) {
      auto sw = __builtin_amdgcn_permlane16_swap(__float_as_uint(sc[w]), __float_as_uint(sc[w + 4]), false, false);
      p[w] = __uint_as_float(sw[0]) + __uint_as_float(sw[1]);
      if (first && n - 16 - (w + 4 * half) < 0) p[w] = -1e30f;
    }
    float mx = fmaxf(fmaxf(p[0], p[1]), fmaxf(p[2], p[3]));
    { auto sw = __builtin_amdgcn_permlane16_swap(__float_as_uint(mx), __float_as_uint(mx), false, false); mx = fmaxf(__uint_as_float(sw[0]), __uint_as_float(sw[1])); }
#pragma unroll
    for (int w = 0; w < 4; ++w) p[w] = __builtin_amdgcn_exp2f(p[w] - mx);
    const float inv = __builtin_amdgcn_rcpf(x16sum((p[0] + p[1]) + (p[2] + p[3])));
    const u32 pa = pk(p[0] * inv, p[1] * inv), pb = pk(p[2] * inv, p[3] * inv);
    auto sa = __builtin_amdgcn_permlane16_swap(pa, pa, false, false);
    auto sb = __builtin_amdgcn_permlane16_swap(pb, pb, false, false);
    const u32 P[4] = {sa[0], sb[0], sa[1], sb[1]};
    float o[8] = {0, 0, 0, 0, 0, 0, 0, 0}, orest = 0.f;
    const u32 sel = half ? 0x07060302u : 0x05040100u;
#pragma unroll
    for (int i = 0; i < 4; ++i) {
      const u32x4 va = lds128(kp + 9216 + (7 - 2 * i) * 16), vb = lds128(kp + 9216 + (6 - 2 * i) * 16);
      const u32 ra = *(const LAS u32*)(rp + 1152 + (7 - 2 * i) * 8), rb = *(const LAS u32*)(rp + 1152 + (6 - 2 * i) * 8);
#pragma unroll
      for (int d = 0; d < 4; ++d) { o[2 * d] = dot2(plo(va[d], vb[d]), P[i], o[2 * d]); o[2 * d + 1] = dot2(phi(va[d], vb[d]), P[i], o[2 * d + 1]); }
      orest = dot2(__builtin_amdgcn_perm(rb, ra, sel), P[i], orest);
    }
    const u32 ob = OFF_OB + L * XB_SZ + (m * 8 + wave) * 18432;
    st128(u32x4{pk(o[0], o[1]), pk(o[2], o[3]), pk(o[4], o[5]), pk(o[6], o[7])}, ws, l16, ob + s * 1024);
    auto s1 = __builtin_amdgcn_permlane16_swap(__float_as_uint(orest), __float_as_uint(orest), false, false);
    const u32 dr = pk(__uint_as_float(s1[0]), __uint_as_float(s1[1]));
    auto s2 = __builtin_amdgcn_permlane32_swap(dr, dr, false, false);
    st64(u32x2{s2[0], s2[1]}, ws, l < 16 ? l16 : OOB, ob + (16 + (s >> 3)) * 1024 + ((s >> 1) & 3) * 256 + (s & 1) * 8);
    WAITV(0);
  };

  if (wave < 4) {
    const int w = wave;
    u32x4 rc0, rc1; u32x2 rcm;
    auto rowoff = [&]() { return (u32)(2 * (128 * (m & 7) + 64 * th + 16 * w + t16) + (m >> 3)) * 1152 + 144 * c8; };
    auto unpack_res = [&](f32x4* res) {
#pragma unroll
      for (int T = 0; T < 4; ++T) res[T] = f32x4{0, 0, 0, 0};
      unchunk_add(res, rc0, 0); unchunk_add(res, rc1, 1);
      res[4] = f32x4{lo16(rcm[0]), hi16(rcm[0]), lo16(rcm[1]), hi16(rcm[1])};
    };
    auto pack_res = [&](const f32x4* res) { rc0 = chunk(res, 0); rc1 = chunk(res, 1); rcm = u32x2{pk(res[4][0], res[4][1]), pk(res[4][2], res[4][3])}; };
    auto publish_x = [&](int xi) {
      const u32 xb = OFF_XB + xi * XB_SZ + (m * 8 + 4 * th + w) * 18432;
      st128(rc0, ws, l16, xb + 2 * c8 * 1024);
      st128(rc1, ws, l16, xb + (2 * c8 + 1) * 1024);
      st64(rcm, ws, mact ? t16 * 16 + (g4 & 1) * 8 : OOB, xb + (16 + (c8 >> 2)) * 1024 + (c8 & 3) * 256);
      f32x4 res[5];
      unpack_res(res);
      float s1 = 0.f, s2 = 0.f;
#pragma unroll
      for (int j = 0; j < 4; ++j) { s1 += res[4][j]; s2 += res[4][j] * res[4][j]; }
      s1 = mact ? s1 : 0.f; s2 = mact ? s2 : 0.f;
#pragma unroll
      for (int T = 0; T < 4; ++T)
#pragma unroll
        for (int j = 0; j < 4; ++j) { s1 += res[T][j]; s2 += res[T][j] * res[T][j]; }
      s1 = x32sum(x16sum(s1)); s2 = x32sum(x16sum(s2));
      st64(u32x2{__float_as_uint(s1), __float_as_uint(s2)}, ws, l < 16 ? l * 8 : OOB, OFF_ST + xi * ST_SZ + ((m * 8 + c8) * 128 + 64 * th + 16 * w) * 8);
    };
    STAMP(0);
    {
      const rsrc_t xr = mkrs(xin, XB_SZ);
      rc0 = __builtin_amdgcn_raw_buffer_load_b128(xr, rowoff() + 32 * g4, 0, 0); rc1 = __builtin_amdgcn_raw_buffer_load_b128(xr, rowoff() + 32 * g4 + 16, 0, 0);
      rcm = __builtin_amdgcn_raw_buffer_load_b64(xr, rowoff() + 128 + 8 * (g4 & 1), 0, 0);
      const int u = w * 16 + m;
      if (u < 36) {
        const rsrc_t qr = mkrs(wqkv, 576 * 1728 * 2);
        const int ks = u < 18 ? u : u - 18;
        const u32 row = kcol0(ks, g4) * 3456 + 72 * s, a = row + (t16 < 8 ? 4 * t16 : 4 + 4 * t16);
        const u32 dst = wt + ks * 7168;
        if (u < 18) {
          const int g = t16 >> 2;
          const u32 ar = g == 3 ? OOB : row + (g == 0 ? 1152 : g == 1 ? 2304 : 0) + 36 * ((t16 & 3) >> 1) + 32 + 2 * (t16 & 1);
          u32 k[8], h[8];
#pragma unroll
          for (int j = 0; j < 8; ++j) { k[j] = __builtin_amdgcn_raw_buffer_load_b32(qr, a, 1152 + j * 3456, 0); h[j] = __builtin_amdgcn_raw_buffer_load_b16(qr, ar, j * 3456, 0); }
          st128(u32x4{plo(k[0], k[1]), plo(k[2], k[3]), plo(k[4], k[5]), plo(k[6], k[7])}, ws, l16, dst);
          st128(u32x4{phi(k[0], k[1]), phi(k[2], k[3]), phi(k[4], k[5]), phi(k[6], k[7])}, ws, l16, dst + 1024);
          st128(u32x4{plo(h[0], h[1]), plo(h[2], h[3]), plo(h[4], h[5]), plo(h[6], h[7])}, ws, l16, dst + 6144);
        } else {
          u32 v[8], q[8];
#pragma unroll
          for (int j = 0; j < 8; ++j) { v[j] = __builtin_amdgcn_raw_buffer_load_b32(qr, a, 2304 + j * 3456, 0); q[j] = __builtin_amdgcn_raw_buffer_load_b32(qr, a, j * 3456, 0); }
          st128(u32x4{plo(v[0], v[1]), plo(v[2], v[3]), plo(v[4], v[5]), plo(v[6], v[7])}, ws, l16, dst + 2048);
          st128(u32x4{phi(v[0], v[1]), phi(v[2], v[3]), phi(v[4], v[5]), phi(v[6], v[7])}, ws, l16, dst + 3072);
          st128(u32x4{plo(q[0], q[1]), plo(q[2], q[3]), plo(q[4], q[5]), plo(q[6], q[7])}, ws, l16, dst + 4096);
          st128(u32x4{phi(q[0], q[1]), phi(q[2], q[3]), phi(q[4], q[5]), phi(q[6], q[7])}, ws, l16, dst + 5120);
        }
      } else if (u < 54) {
        const rsrc_t orr = mkrs(wo, 576 * 576 * 2);
        const int ks = u - 36;
        const u32 row = (ks < 16 ? 36 * ks + 18 * (g4 >> 1) + 8 * (g4 & 1) : 36 * (8 * (ks - 16) + 2 * g4) + 16) * 1152;
        const u32 a = row + 144 * c8 + 8 * t16, am = row + 288 * r + (t16 < 8 ? 128 + 2 * t16 : 256 + 2 * t16);
        u32x2 v[8]; u32 h[8];
#pragma unroll
        for (int j = 0; j < 8; ++j) {
          const u32 so = (ks < 16 ? j : 36 * (j >> 2) + 18 * ((j & 3) >> 1) + (j & 1)) * 1152;
          v[j] = __builtin_amdgcn_raw_buffer_load_b64(orr, a, so, 0); h[j] = __builtin_amdgcn_raw_buffer_load_b16(orr, am, so, 0);
        }
        const u32 dst = wt + WT_O + ks * 5120;
#pragma unroll
        for (int d = 0; d < 2; ++d) {
          st128(u32x4{plo(v[0][d], v[1][d]), plo(v[2][d], v[3][d]), plo(v[4][d], v[5][d]), plo(v[6][d], v[7][d])}, ws, l16, dst + 2 * d * 1024);
          st128(u32x4{phi(v[0][d], v[1][d]), phi(v[2][d], v[3][d]), phi(v[4][d], v[5][d]), phi(v[6][d], v[7][d])}, ws, l16, dst + (2 * d + 1) * 1024);
        }
        st128(u32x4{plo(h[0], h[1]), plo(h[2], h[3]), plo(h[4], h[5]), plo(h[6], h[7])}, ws, l16, dst + 4096);
      }
      STAMP(16);
      publish_x(0);
      STAMP(17);
      PIN(rc0); PIN(rc1); PIN(rcm);
      WAITV(0);
      STAMP(1);
      BAR();
    }
    for (int L = 0; L < 2; ++L) {
      BAR();
      relane();
      STAMP(2 + 16 * L);
      {
        const u32 sb = OFF_ST + 2 * L * ST_SZ + (m * 8 * 128 + 32 * w) * 8, sv = g4 * 2048 + t16 * 8;
        u32x2 st[4];
#pragma unroll
        for (int e = 0; e < 2; ++e) { st[2 * e] = ld64(ws, sv, sb + e * 128); st[2 * e + 1] = ld64(ws, sv, sb + e * 128 + 1024); }
        const u32 xb = OFF_XB + 2 * L * XB_SZ + (m * 8 + 2 * w) * 18432;
        u32x4 xq[18][2];
#pragma unroll
        for (int ks = 0; ks < 14; ++ks) { xq[ks][0] = ldfrag(ws, l16, xb, ks * 1024); xq[ks][1] = ldfrag(ws, l16, xb + 18432, ks * 1024); }
        BAR();
        STAMP(3 + 16 * L);
        f32x4 acc[7][2];
        u32x4 a[7];
        int o1 = 64512; OPQ(o1);
        const LAS char* w0 = lds + l16; const LAS char* w1p = w0 + o1;
#pragma unroll
        for (int T = 0; T < 7; ++T) a[T] = lds128(w0 + T * 1024);
        SB();
#pragma unroll
        for (int ks = 0; ks < 18; ++ks)
#pragma unroll
          for (int T = 0; T < 7; ++T) {
            mac16(acc[T][0], a[T], xq[ks][0], ks == 0);
            mac16(acc[T][1], a[T], xq[ks][1], ks == 0);
            SB();
            if (ks < 4 && T == 6) { xq[ks + 14][0] = ldfrag(ws, l16, xb, (ks + 14) * 1024); xq[ks + 14][1] = ldfrag(ws, l16, xb + 18432, (ks + 14) * 1024); }
            if (ks < 17) a[T] = ks + 1 < 9 ? lds128(w0 + ((ks + 1) * 7 + T) * 1024) : lds128(w1p + ((ks - 8) * 7 + T) * 1024);
            SB();
          }
        MFMA_DONE();
#pragma unroll
        for (int T = 0; T < 7; ++T) { PIN(acc[T][0]); PIN(acc[T][1]); }
        float mean[2], rstd[2];
        stats16(st[0], st[1], mean[0], rstd[0]); stats16(st[2], st[3], mean[1], rstd[1]);
        STAMP(4 + 16 * L);
        BAR();
      relane();
        f32x4 cs[7];
#pragma unroll
        for (int T = 0; T < 7; ++T) cs[T] = *(const LAS f32x4*)(lds + L_CSQ + T * 64 + g4 * 16);
#pragma unroll
        for (int e = 0; e < 2; ++e) {
          const float qs = rstd[e] * (0.23570226039551584f * LOG2E), rs6 = g4 == 2 ? qs : rstd[e];
          const int n = 16 + 32 * w + 16 * e + t16;
          f32x4 v[7];
#pragma unroll
          for (int T = 0; T < 7; ++T) v[T] = (acc[T][e] - mean[e] * cs[T]) * (T < 4 ? rstd[e] : T < 6 ? qs : rs6);
          *(LAS u32x4*)(lds + L_KS + g4 * 2304 + n * 16) = u32x4{pk(v[0][0], v[1][0]), pk(v[0][1], v[1][1]), pk(v[0][2], v[1][2]), pk(v[0][3], v[1][3])};
          *(LAS u32x4*)(lds + L_VS + g4 * 2304 + n * 16) = u32x4{pk(v[2][0], v[3][0]), pk(v[2][1], v[3][1]), pk(v[2][2], v[3][2]), pk(v[2][3], v[3][3])};
          *(LAS u32x4*)(lds + L_QS + g4 * 2048 + (n - 16) * 16) = u32x4{pk(v[4][0], v[5][0]), pk(v[4][1], v[5][1]), pk(v[4][2], v[5][2]), pk(v[4][3], v[5][3])};
          if (g4 < 3) *(LAS u32x2*)(lds + L_RS + g4 * 1152 + n * 8) = u32x2{pk(v[6][0], v[6][1]), pk(v[6][2], v[6][3])};
        }
        WAITL();
        BAR();
      }
      STAMP(5 + 16 * L);
      attention(L);
      STAMP(6 + 16 * L);
      BAR();
      BAR();
      relane();
      STAMP(7 + 16 * L);
      {
        const u32 ob = OFF_OB + L * XB_SZ + (m * 8 + 4 * th + w) * 18432;
        u32x4 of[18];
#pragma unroll
        for (int ks = 0; ks < 18; ++ks) of[ks] = ldfrag(ws, l16, ob, ks * 1024);
        u32x4 a[10];
        f32x4 res[5];
        unpack_res(res);
        int o1 = 46080; OPQ(o1);
        const LAS char* w0 = lds + l16; const LAS char* w1p = w0 + o1;
#pragma unroll
        for (int i = 0; i < 10; ++i) a[i] = lds128(w0 + i * 1024);
#pragma unroll
        for (int T = 0; T < 5; ++T) PIN(res[T]);
        asm volatile("s_nop 1");
        SB();
#pragma unroll
        for (int i = 0; i < 90; ++i) {
          mac16(res[i % 5], a[i % 10], of[i / 5], false);
          SB();
          if (i + 10 < 90) a[i % 10] = i + 10 < 45 ? lds128(w0 + (i + 10) * 1024) : lds128(w1p + (i - 35) * 1024);
          SB();
        }
        MFMA_DONE();
#pragma unroll
        for (int T = 0; T < 5; ++T) PIN(res[T]);
        pack_res(res);
        PIN(rc0); PIN(rc1); PIN(rcm);
        publish_x(2 * L + 1);
        PIN(rc0); PIN(rc1); PIN(rcm);
        WAITV(0);
      }
      STAMP(8 + 16 * L);
      BAR();
      BAR();
      relane();
      STAMP(9 + 16 * L);
      float rstd_own;
      {
        const int tok = l & 31, hh = l >> 5;
        const u32 sb = OFF_ST + (2 * L + 1) * ST_SZ + m * 8 * 128 * 8;
        u32x2 st[4], so2[2];
#pragma unroll
        for (int i = 0; i < 4; ++i) st[i] = ld64(ws, hh * 4096 + tok * 8, sb + 32 * w * 8 + i * 1024);
#pragma unroll
        for (int i = 0; i < 2; ++i) so2[i] = ld64(ws, g4 * 2048 + t16 * 8, sb + (64 * th + 16 * w) * 8 + i * 1024);
        const u32 xb = OFF_XB + (2 * L + 1) * XB_SZ + (m * 8 + 2 * w) * 18432, xv = (tok >> 4) * 18432 + hh * 256 + t16 * 16;
        u32x4 x[36];
#pragma unroll
        for (int u = 0; u < 36; ++u) x[u] = ldfrag(ws, xv, xb, u * 512);
        u32x4 a[6];
        const LAS char* lb = lds + l16;
        int so = 3 * SLOT, sn = 0;
        OPQ(so); OPQ(sn);
#pragma unroll
        for (int i = 0; i < 6; ++i) a[i] = lds128(lb + so + i * 1024);
        float mean, rstd;
        {
          float s1 = 0.f, s2 = 0.f;
#pragma unroll
          for (int i = 0; i < 4; ++i) { s1 += __uint_as_float(st[i][0]); s2 += __uint_as_float(st[i][1]); }
          fin_stats(x32sum(s1), x32sum(s2), mean, rstd);
          float mo; stats16(so2[0], so2[1], mo, rstd_own);
        }
        const float c2 = -LOG2E * rstd;
        u32x4 bm = {0, 0, 0, 0}, acs = {0, 0, 0, 0};
        { const u32 mh = pk(-mean, -mean); const float ml = -mean - hi16(mh); bm[0] = hh ? pk(ml, ml) : mh; }
        const LAS char* csp = lds + L_CS1 + tok * 4;
        acs[0] = *(const LAS u32*)csp;
        u32 hoff = OFF_HB + L * HB_SZ + cu * 73728 + w * 2048;
        const u32 hsrc = OFF_HB + L * HB_SZ + (m * 16 + 4 * qd) * 73728 + w * 2048, hv = (g4 & 1) * 1024 + (g4 >> 1) * 512 + t16 * 16;
        f32x16 acc0, acc1;
        u32x4 hb[2][8];
        f32x4 c[9][2];
        u32x4 h0, h1; float te[2], ty[2], tm, ta, tr;
        auto silu_gap = [&](int i, f32x16& prev) {
          if (i == 0) PIN(prev);
          const int e = i >> 1;
          if ((i & 1) == 0) {
            if (e >= 2) { tr = __builtin_amdgcn_rcpf(ta); PIN(tr); }
            if (e < 16) { tm = prev[e] * c2; PIN(tm); }
            if (e >= 4 && (e & 1) == 0) { const int f = (e - 4) >> 1; u32 p = pk(ty[0], ty[1]); PIN(p); if (f < 4) h0[f] = p; else h1[f - 4] = p; }
          } else {
            if (e < 16) { te[e & 1] = __builtin_amdgcn_exp2f(tm); PIN(te[e & 1]); }
            if (e >= 2) { ty[e & 1] = prev[e - 2] * tr; PIN(ty[e & 1]); }
            if (e >= 1 && e < 17) { ta = 1.f + te[(e - 1) & 1]; PIN(ta); }
          }
          if (i == 24) st128(h0, ws, l16, hoff);
          if (i == 35) { h1[3] = pk(ty[0], ty[1]); st128(h1, ws, l16 + 1024, hoff); hoff += 8192; }
        };
        auto advance = [&]() { so = sn; sn = sn == 3 * SLOT ? 0 : sn + SLOT; asm volatile("" : "+s"(so), "+s"(sn)); };
        auto f1step = [&](auto mode, f32x16& cur, f32x16& prev) {
          constexpr int MODE = decltype(mode)::value;
          const LAS char* base = lb + so; const LAS char* nbase = lb + sn;
          mul32(cur, acs, bm);
          SB();
          if (MODE != 2) { csp += 128; acs[0] = *(const LAS u32*)csp; }
          SB();
#pragma unroll
          for (int i = 0; i < 36; ++i) {
            const int g = i / 3, u = i % 3, bank = (g & 1) * 3, p = ((g >> 1) & 1) ? u : 2 - u;
            mac32(cur, a[bank + p], x[g * 3 + p]);
            SB();
            { const int nk = (g + 2) * 3 + p; a[bank + p] = nk < 36 ? lds128(base + nk * 1024) : lds128(nbase + (nk - 36) * 1024); }
            if (MODE) silu_gap(i, prev);
            if (MODE == 2 && i >= 4 && (i & 1) == 0) { const int k = (i - 4) >> 1; hb[k >> 3][k & 7] = ld128(ws, hv + (k & 1) * 256, hsrc + ((k & 7) >> 1) * 73728 + (k >> 3) * 8192); }
            if (i == 29) { if (MODE == 1) WAITV(1); BAR(); }
            SB();
          }
          advance();
        };
        auto f2step = [&](auto mode, auto par, u32 hnext) {
          constexpr int MODE = decltype(mode)::value, P = decltype(par)::value;
          const LAS char* base = lb + so; const LAS char* nbase = lb + sn;
#pragma unroll
          for (int i = 0; i < 36; ++i) {
            const int g = i / 3, u = i % 3, bank = (g & 1) * 3, p = ((g >> 1) & 1) ? u : 2 - u;
            const int f = g * 3 + p, kk = f / 9, T = f % 9;
            mac16(c[T][0], a[bank + p], hb[P][2 * kk], MODE == 0 && kk == 0);
            mac16(c[T][1], a[bank + p], hb[P][2 * kk + 1], MODE == 0 && kk == 0);
            if (MODE == 0 && i == 0) asm volatile("s_nop 7");
            SB();
            { const int nk = (g + 2) * 3 + p; if (nk < 36) a[bank + p] = lds128(base + nk * 1024); else if (MODE != 4) a[bank + p] = lds128(nbase + (nk - 36) * 1024); }
            if (MODE == 0) silu_gap(i, acc0);
            if (MODE < 3) {
              if (i % 9 == 8) hb[P][2 * (i / 9)] = ld128(ws, hv, hnext + (i / 9) * 73728);
              if (i % 9 == 3 && i > 9) hb[P][2 * (i / 9) - 1] = ld128(ws, hv + 256, hnext + (i / 9 - 1) * 73728);
              if (i == 35) hb[P][7] = ld128(ws, hv + 256, hnext + 3 * 73728);
            }
            if (i == 29 && MODE != 4) { if (MODE == 1) WAITV(5); BAR(); }
            SB();
          }
          advance();
        };
        using I0 = std::integral_constant<int, 0>; using I1 = std::integral_constant<int, 1>; using I2 = std::integral_constant<int, 2>;
        using I3 = std::integral_constant<int, 3>; using I4 = std::integral_constant<int, 4>;
        PIN(rstd_own); PIN(bm); PIN(acs);
        asm volatile("s_nop 1");
        SB();
        f1step(I0{}, acc0, acc1);
        STAMP(10 + 16 * L);
        f1step(I1{}, acc1, acc0);
#pragma unroll 1
        for (int j = 0; j < 3; ++j) { f1step(I1{}, acc0, acc1); f1step(I1{}, acc1, acc0); }
        f1step(I2{}, acc0, acc1);
        STAMP(11 + 16 * L);
        f2step(I0{}, I0{}, hsrc + 2 * 8192);
        f2step(I1{}, I1{}, hsrc + 3 * 8192);
        f2step(I2{}, I0{}, hsrc + 4 * 8192);
#pragma unroll 1
        for (int j = 0; j < 2; ++j) { f2step(I2{}, I1{}, hsrc + (5 + 2 * j) * 8192); f2step(I2{}, I0{}, hsrc + (6 + 2 * j) * 8192); }
        f2step(I3{}, I1{}, 0);
        f2step(I4{}, I0{}, 0);
        STAMP(12 + 16 * L);
        MFMA_DONE();
#pragma unroll
        for (int T = 0; T < 9; ++T) { PIN(c[T][0]); PIN(c[T][1]); }
        const u32 pb = OFF_PB + L * PB_SZ + (((m * 4 + r) * 4 + qd) * 4 + w) * 9216;
#pragma unroll
        for (int e = 0; e < 2; ++e)
#pragma unroll
          for (int hc = 0; hc < 2; ++hc) {
            const f32x4 t[4] = {c[4 * hc][e], c[4 * hc + 1][e], c[4 * hc + 2][e], c[4 * hc + 3][e]};
            st128(chunk(t, 0), ws, l16, pb + (e * 4 + hc * 2) * 1024);
            st128(chunk(t, 1), ws, l16, pb + (e * 4 + hc * 2 + 1) * 1024);
          }
        st128(u32x4{pk(c[8][0][0], c[8][0][1]), pk(c[8][0][2], c[8][0][3]), pk(c[8][1][0], c[8][1][1]), pk(c[8][1][2], c[8][1][3])}, ws, l16, pb + 8192);
        WAITV(0);
      }
      STAMP(13 + 16 * L);
      BAR();
      BAR();
      relane();
      STAMP(14 + 16 * L);
      {
        const u32 pb = OFF_PB + L * PB_SZ + ((m * 4 + r) * 16 + 2 * th + (w >> 1)) * 9216;
        u32x4 pc[4][2]; u32x2 pm[4];
#pragma unroll
        for (int k = 0; k < 4; ++k) {
          pc[k][0] = ld128(ws, l16, pb + k * 36864 + ((w & 1) * 4 + ch * 2) * 1024);
          pc[k][1] = ld128(ws, l16, pb + k * 36864 + ((w & 1) * 4 + ch * 2 + 1) * 1024);
          pm[k] = ld64(ws, l16, pb + k * 36864 + 8192 + (w & 1) * 8);
        }
        f32x4 t[5];
#pragma unroll
        for (int T = 0; T < 5; ++T) t[T] = f32x4{0, 0, 0, 0};
#pragma unroll
        for (int k = 0; k < 4; ++k) {
          unchunk_add(t, pc[k][0], 0); unchunk_add(t, pc[k][1], 1);
          t[4] += f32x4{lo16(pm[k][0]), hi16(pm[k][0]), lo16(pm[k][1]), hi16(pm[k][1])};
        }
        f32x4 res[5];
        unpack_res(res);
#pragma unroll
        for (int T = 0; T < 5; ++T) res[T] += rstd_own * t[T];
        pack_res(res);
      }
      if (L == 0) {
        publish_x(2);
        PIN(rc0); PIN(rc1); PIN(rcm);
        WAITV(0);
        STAMP(15);
        BAR();
      }
    }
    const rsrc_t orr = mkrs(out, XB_SZ);
    relane();
    __builtin_amdgcn_raw_buffer_store_b128(rc0, orr, rowoff() + 32 * g4, 0, 0);
    __builtin_amdgcn_raw_buffer_store_b128(rc1, orr, rowoff() + 32 * g4 + 16, 0, 0);
    __builtin_amdgcn_raw_buffer_store_b64(rcm, orr, mact ? rowoff() + 128 + 8 * (g4 & 1) : OOB, 0, 0);
    STAMP(31);
  } else {
    const int hw = wave - 4;
    const int cons = hw == 3 ? cu : g4 == 0 ? m * 16 + t16 : g4 == 1 ? ((m + 1) & 15) * 16 + t16 : t16 * 16 + s;
    u32x2 tag = {0, 0};
    if (hw >= 2) tag = ld64(ws, cons * 128, OFF_NONCE);
    const u32 mail = OFF_MAIL + cons * 4096;
    u32x2 ra[2][8], rb[2][8]; u32 rh[2][8];
    const int un0 = hw * 16 + m;
    auto unit_load = [&](int k, int un) {
      const bool is1 = un < 36;
      const rsrc_t sr = mkrs(is1 ? w1 : w2, 576 * 4608 * 2);
      const int i = l & 31, j8 = (un - 36) >> 2, i0 = 16 * (g4 & 1) + 4 * (g4 >> 1);
      const u32 row1 = kcol0(un >> 1, 2 * (un & 1) + (l >> 5)) * 9216 + 576 * s;
      const u32 row2 = (288 * (4 * qd + (un & 3)) + (j8 < 8 ? 8 * i0 + j8 : 256 + i0)) * 1152 + 288 * r;
      const u32 a0 = is1 ? row1 + 16 * i : row2 + 8 * t16, a1 = a0 + (is1 ? 8 : 144);
      const u32 a2 = is1 ? row1 + 512 + 2 * i : row2 + (t16 < 8 ? 128 + 2 * t16 : 256 + 2 * t16);
      u32 sA = j8 < 8 ? 9216 : 1152, sB = is1 ? 36864 : 8 * sA;
      OPQ(sA); OPQ(sB);
#pragma unroll
      for (int j = 0; j < 8; ++j) {
        const u32 so = (j & 3) * sA + (j >> 2) * sB;
        ra[k][j] = __builtin_amdgcn_raw_buffer_load_b64(sr, a0, so, 0);
        rb[k][j] = __builtin_amdgcn_raw_buffer_load_b64(sr, a1, so, 0);
        rh[k][j] = __builtin_amdgcn_raw_buffer_load_b16(sr, a2, so, 0);
      }
    };
    auto unit_store = [&](int k, int un) {
      const bool is1 = un < 36;
      u32 dst = wt + WT_F + (is1 ? un : 324 + (un - 36) * 9) * 1024, csd = OFF_CS + s * CS_PER + un * 1152; const u32 step = is1 ? 36864 : 1024;
      OPQ(dst); OPQ(csd);
#pragma unroll
      for (int j = 0; j < 8; ++j) { PIN(ra[k][j]); PIN(rb[k][j]); PIN(rh[k][j]); }
#pragma unroll
      for (int T = 0; T < 9; ++T) {
        u32x4 f;
#pragma unroll
        for (int j = 0; j < 4; ++j) {
          const u32 x0 = T == 8 ? rh[k][2 * j] : T < 4 ? ra[k][2 * j][(T >> 1) & 1] : rb[k][2 * j][(T >> 1) & 1];
          const u32 x1 = T == 8 ? rh[k][2 * j + 1] : T < 4 ? ra[k][2 * j + 1][(T >> 1) & 1] : rb[k][2 * j + 1][(T >> 1) & 1];
          f[j] = (T & 1) ? phi(x0, x1) : plo(x0, x1);
        }
        st128(f, ws, l16, dst + T * step);
        if (is1) {
          const float cs = x32sum(dot2(f[3], ONES, dot2(f[2], ONES, dot2(f[1], ONES, dot2(f[0], ONES, 0.f)))));
          __builtin_amdgcn_raw_buffer_store_b32(__float_as_uint(cs), ws, (l < 32 ? l * 4 : OOB) + T * 128, csd, SC1);
        }
      }
    };
#pragma unroll
    for (int k = 0; k < 2; ++k)
#pragma unroll
      for (int j = 0; j < 8; ++j) { ra[k][j] = u32x2{0, 0}; rb[k][j] = u32x2{0, 0}; rh[k][j] = 0; }
    if (hw >= 2) { tag = next_tag(tag); PIN(tag); }
    if (hw < 3) { unit_load(0, un0); if (un0 < 24) unit_load(1, 48 + un0); }
    BAR();
    for (int L = 0; L < 2; ++L) {
      const u32 ph = L * 2048;
      relane();
      if (hw == 2) st64(tag, ws, g4 == 0 ? mail + s * 8 : g4 == 1 ? (th == 1 && (m & 7) != 7 ? mail + (16 + c8) * 8 : OOB) : (g4 == 2 && L == 0 ? mail + 256 + m * 8 : OOB), ph);
      if (hw == 3) poll(ws, mail + l * 8, ph, l < 16 || (l < 24 && !first) || (g4 == 2 && L == 0), tag);
      else if (L == 0) { unit_store(0, un0); if (un0 < 24) unit_store(1, 48 + un0); }
      BAR();
      if (hw < 2 && L == 0) dma9<0>(ws, smem, hw * 64512, l16, wt + hw * 64512, 7);
      u32x4 xh[18]; u32x2 st[2];
      {
        const u32 pm = first ? OOB : 0;
        const u32 sb = OFF_ST + 2 * L * ST_SZ + ((m - 1) * 8 * 128 + 112) * 8;
        st[0] = ld64(ws, pm + g4 * 2048 + t16 * 8, sb); st[1] = ld64(ws, pm + g4 * 2048 + t16 * 8, sb + 1024);
        const u32 xb = OFF_XB + 2 * L * XB_SZ + (m * 8 - 1) * 18432, xv = (first || t16 < 8) ? OOB : l16;
#pragma unroll
        for (int ks = 0; ks < 18; ++ks) xh[ks] = ldfrag(ws, xv, xb, ks * 1024);
      }
      WAITV(20);
      BAR();
      f32x4 acc[2] = {f32x4{0, 0, 0, 0}, f32x4{0, 0, 0, 0}};
      {
        int o1 = 64512; OPQ(o1);
        const LAS char* w0 = lds + l16 + hw * 2048; const LAS char* w1p = w0 + o1;
        const int d1 = hw == 3 ? 0 : 1024;
        __builtin_amdgcn_s_setprio(1);
        u32x4 a0[18], a1[18];
#pragma unroll
        for (int ks = 0; ks < 4; ++ks) { a0[ks] = lds128(w0 + ks * 7168); a1[ks] = lds128(w0 + ks * 7168 + d1); }
        SB();
#pragma unroll
        for (int ks = 0; ks < 18; ++ks) {
          u32x4 xv = xh[ks];
#pragma unroll
          for (int j = 0; j < 4; ++j) xv[j] = t16 == 0 ? ONES : xv[j];
          acc[0] = mfma16(a0[ks], xv, acc[0]);
          acc[1] = mfma16(a1[ks], xv, acc[1]);
          if (ks + 4 < 18) { const LAS char* p = ks + 4 < 9 ? w0 + (ks + 4) * 7168 : w1p + (ks - 5) * 7168; a0[ks + 4] = lds128(p); a1[ks + 4] = lds128(p + d1); }
          SB();
        }
        __builtin_amdgcn_s_setprio(0);
        if (t16 == 0) {
          *(LAS f32x4*)(lds + L_CSQ + hw * 128 + g4 * 16) = acc[0];
          if (hw != 3) *(LAS f32x4*)(lds + L_CSQ + hw * 128 + 64 + g4 * 16) = acc[1];
        }
      }
      float mean, rstd;
      stats16(st[0], st[1], mean, rstd);
      WAITL();
      BAR();
      if (hw != 2) {
        const f32x4 c0 = *(const LAS f32x4*)(lds + L_CSQ + hw * 128 + g4 * 16), c1 = *(const LAS f32x4*)(lds + L_CSQ + (hw == 3 ? 0 : hw * 128 + 64) + g4 * 16);
        const f32x4 v0 = (acc[0] - mean * c0) * rstd, v1 = (acc[1] - mean * c1) * rstd;
        if (hw < 2) *(LAS u32x4*)(lds + L_KS + hw * 9216 + g4 * 2304 + t16 * 16) = u32x4{pk(v0[0], v1[0]), pk(v0[1], v1[1]), pk(v0[2], v1[2]), pk(v0[3], v1[3])};
        else if (g4 < 2) *(LAS u32x2*)(lds + L_RS + g4 * 1152 + t16 * 8) = u32x2{pk(v0[0], v0[1]), pk(v0[2], v0[3])};
      }
      WAITL();
      BAR();
      attention(L);
      BAR();
      if (hw < 2) { dma9<0>(ws, smem, hw * 46080, l16, wt + WT_O + hw * 46080, 5); WAITV(0); }
      if (hw == 2) st64(tag, ws, g4 == 0 ? mail + s * 8 : (g4 == 2 && L == 0 ? mail + 256 + m * 8 : OOB), ph + 512);
      if (hw == 3) poll(ws, mail + l * 8, ph + 512, l < 16 || (g4 == 2 && L == 0), tag);
      BAR();
      if (hw < 2) { __builtin_amdgcn_s_sleep(16); dma9<1>(ws, smem, 3 * SLOT + hw * 18432, l16, wt + WT_F + hw * 18432, 2); }
      else if (L == 0) {
        const u32 cv = l < 36 ? ((hw - 2) * 36 + l) * 16 : OOB;
        u32 csb = OFF_CS + s * CS_PER; OPQ(csb);
        f32x4 sum = {0, 0, 0, 0};
        u32x4 part[36];
#pragma unroll
        for (int k = 0; k < 36; ++k) part[k] = ld128(ws, cv + (k % 3) * 1152, csb + (k / 3) * 3456);
        SB();
#pragma unroll
        for (int k = 0; k < 36; ++k) sum += __builtin_bit_cast(f32x4, part[k]);
        u32x4 o;
#pragma unroll
        for (int j = 0; j < 4; ++j) { const float h = hi16(pk(sum[j], sum[j])); o[j] = pk(h, sum[j] - h); }
        if (l < 36) *(LAS u32x4*)(lds + L_CS1 + cv) = o;
        WAITL();
      }
      BAR();
      if (hw < 2) {
#pragma unroll 1
        for (int n = 1; n < 4; ++n) dma9<0>(ws, smem, (n - 1) * SLOT + hw * 18432, l16, wt + WT_F + n * SLOT + hw * 18432, 2);
        WAITV(54);
      }
      if (hw == 2) st64(tag, ws, g4 == 0 ? mail + s * 8 : OOB, ph + 1024);
      if (hw == 3) poll(ws, mail + l * 8, ph + 1024, l < 16, tag);
      BAR();
      if (hw < 2) {
        WAITV(36);
        BAR();
        int sf = 3 * SLOT;
#pragma unroll 1
        for (int n = 1; n < 15; ++n) {
          WAITV(18);
          BAR();
          dma9<1>(ws, smem, sf + hw * 18432, l16, wt + WT_F + (n + 3) * SLOT + hw * 18432, 2);
          sf = sf == 3 * SLOT ? 0 : sf + SLOT;
        }
        WAITV(18); BAR();
        WAITV(0); BAR();
      } else {
#pragma unroll 1
        for (int n = 0; n < 17; ++n) {
          if (hw == 3 && (n == 7 || n == 9 || n == 12)) { const int k = n == 7 ? 0 : n == 9 ? 1 : 2; poll(ws, mail + l * 8, ph + 1280, (l >> 2) == k, tag); }
          BAR();
          if (hw == 2 && (n == 4 || n == 7 || n == 10)) st64(tag, ws, g4 == 0 && (t16 >> 2) == qd ? mail + (4 * ((n - 4) / 3) + r) * 8 : OOB, ph + 1280);
        }
      }
      BAR();
      if (hw == 2) st64(tag, ws, g4 == 0 && (t16 & 3) == r ? mail + qd * 8 : OOB, ph + 1536);
      if (hw == 3) poll(ws, mail + l * 8, ph + 1536, l < 4, tag);
      if (hw < 2 && L == 0) dma9<0>(ws, smem, hw * 64512, l16, wt + hw * 64512, 7);
      BAR();
      if (L == 0) BAR();
    }
    if (hw == 3) st64(tag, ws, l == 0 ? cu * 128 : OOB, OFF_NONCE);
  }
}
}

extern "C" void kernel_launch(void* const* d_in, const int* in_sizes, int n_in, void* d_out, int out_size, void* d_ws,
                              size_t ws_size, hipStream_t stream) {
  (void)in_sizes; (void)n_in; (void)out_size; (void)ws_size;
  (void)hipFuncSetAttribute((const void*)fused_fwd, hipFuncAttributeMaxDynamicSharedMemorySize, LDS_BYTES);
  fused_fwd<<<256, 512, LDS_BYTES, stream>>>((const u16*)d_in[0], (const u16*)d_in[1], (const u16*)d_in[2], (const u16*)d_in[4],
                                               (const u16*)d_in[5], (u16*)d_out, (char*)d_ws);
}
```

```cpp
#include <hip/hip_runtime.h>
#include <hip/hip_bf16.h>
#include <type_traits>
#include <utility>

namespace {
typedef unsigned short u16;
typedef unsigned u32;
typedef unsigned long long u64;
typedef __attribute__((ext_vector_type(2))) unsigned u32x2;
typedef __attribute__((ext_vector_type(4))) unsigned u32x4;
typedef __attribute__((ext_vector_type(2))) __bf16 bf16x2;
typedef __attribute__((ext_vector_type(8))) __bf16 bf16x8;
typedef __attribute__((ext_vector_type(2))) float f32x2;
typedef __attribute__((ext_vector_type(4))) float f32x4;
typedef __attribute__((ext_vector_type(16))) float f32x16;
typedef __amdgpu_buffer_rsrc_t rsrc_t;
template <int N> using IC = std::integral_constant<int, N>;
#define LAS __attribute__((address_space(3)))
#define DEVI __device__ __forceinline__
#define PIN(v) asm volatile("" : "+v"(v))
#define OPQ(v) asm volatile("" : "+s"(v))
#define SB() __builtin_amdgcn_sched_barrier(0)
#define BAR() do { SB(); asm volatile("s_barrier" ::: "memory"); SB(); } while (0)
#define WAITV(n) asm volatile("s_waitcnt vmcnt(" #n ")" ::: "memory")
#define WAITL() asm volatile("s_waitcnt lgkmcnt(0)" ::: "memory")

template <class F, int... I> DEVI void sfor_(F&& f, std::integer_sequence<int, I...>) { (f(IC<I>{}), ...); }
template <int N, class F> DEVI void sfor(F&& f) { sfor_(f, std::make_integer_sequence<int, N>{}); }
constexpr int SC1 = 16;
constexpr u32 OOB = 0x7ff00000u;
constexpr u32 OFF_NONCE = 0, OFF_MAIL = 32768, OFF_WT = OFF_MAIL + 256 * 4096;
constexpr u32 WT_PER = 864 * 1024, WT_O = 126 * 1024, WT_F = 216 * 1024;
constexpr u32 OFF_CS = OFF_WT + 16 * WT_PER, CS_PER = 18 * 1152;
constexpr u32 XB_SZ = 2048 * 576 * 2, OFF_XB = OFF_CS + 16 * CS_PER;
constexpr u32 ST_SZ = 131072, OFF_ST = OFF_XB + 4 * XB_SZ;
constexpr u32 OFF_OB = OFF_ST + 4 * ST_SZ;
constexpr u32 HB_SZ = 2048 * 4608 * 2, OFF_HB = OFF_OB + 2 * XB_SZ;
constexpr u32 PB_SZ = 4 * XB_SZ, OFF_PB = OFF_HB + 2 * HB_SZ;
constexpr u32 OFF_PROF = OFF_PB + 2 * PB_SZ, WS_END = OFF_PROF + 256 * 1024;
constexpr int SLOT = 36864;
constexpr int L_KS = 129024, L_VS = L_KS + 9216, L_QS = L_VS + 9216, L_RS = L_QS + 8192, L_CS1 = L_RS + 3456, L_CSQ = L_CS1 + 1152;
constexpr int L_DUMP = 163328, L_OWN = 4 * SLOT, LDS_BYTES = 163840;
constexpr int QW = 4, FW = 6;
constexpr float LOG2E = 1.4426950408889634f;
constexpr u32 ONES = 0x3F803F80u;

DEVI u32 pk(float a, float b) { f32x2 v = {a, b}; return __builtin_bit_cast(u32, __builtin_convertvector(v, bf16x2)); }
DEVI float lo16(u32 u) { return __uint_as_float(u << 16); }
DEVI float hi16(u32 u) { return __uint_as_float(u & 0xffff0000u); }
DEVI float dot2(u32 a, u32 b, float c) { return __builtin_amdgcn_fdot2_f32_bf16(__builtin_bit_cast(bf16x2, a), __builtin_bit_cast(bf16x2, b), c, false); }
DEVI float x16sum(float v) { auto s = __builtin_amdgcn_permlane16_swap(__float_as_uint(v), __float_as_uint(v), false, false); return __uint_as_float(s[0]) + __uint_as_float(s[1]); }
DEVI float x32sum(float v) { auto s = __builtin_amdgcn_permlane32_swap(__float_as_uint(v), __float_as_uint(v), false, false); return __uint_as_float(s[0]) + __uint_as_float(s[1]); }
DEVI u32 plo(u32 a, u32 b) { return __builtin_amdgcn_perm(b, a, 0x05040100u); }
DEVI u32 phi(u32 a, u32 b) { return __builtin_amdgcn_perm(b, a, 0x07060302u); }
DEVI f32x4 mfma16(u32x4 a, u32x4 b, f32x4 c) { return __builtin_amdgcn_mfma_f32_16x16x32_bf16(__builtin_bit_cast(bf16x8, a), __builtin_bit_cast(bf16x8, b), c, 0, 0, 0); }
DEVI void mac16(f32x4& c, u32x4 a, u32x4 b, bool zero) {
  if (zero) asm volatile("v_mfma_f32_16x16x32_bf16 %0, %1, %2, 0" : "=&v"(c) : "v"(a), "v"(b));
  else asm volatile("v_mfma_f32_16x16x32_bf16 %0, %1, %2, %0" : "+v"(c) : "v"(a), "v"(b));
}
DEVI void mul16x4(f32x4* c, const u32x2* a, const u32x2* b) {
  asm volatile("s_nop 1\n\tv_mfma_f32_16x16x16_bf16 %0, %4, %6, 0\n\tv_mfma_f32_16x16x16_bf16 %2, %4, %7, 0\n\t"
               "v_mfma_f32_16x16x16_bf16 %1, %5, %6, 0\n\tv_mfma_f32_16x16x16_bf16 %3, %5, %7, 0"
               : "=&v"(c[0]), "=&v"(c[1]), "=&v"(c[2]), "=&v"(c[3]) : "v"(a[0]), "v"(a[1]), "v"(b[0]), "v"(b[1]));
}
#define USE(v) asm volatile("" :: "v"(v))
#define MFMA_DONE() asm volatile("s_nop 15")
DEVI u32x4 ld128(rsrc_t r, u32 v, u32 s) { return __builtin_amdgcn_raw_buffer_load_b128(r, v, s, SC1); }
DEVI u32x2 ld64(rsrc_t r, u32 v, u32 s) { return __builtin_amdgcn_raw_buffer_load_b64(r, v, s, SC1); }
DEVI void st128(u32x4 d, rsrc_t r, u32 v, u32 s) { __builtin_amdgcn_raw_buffer_store_b128(d, r, v, s, SC1); asm volatile("s_nop 1" :: "v"(d) : "memory"); }
DEVI void st64(u32x2 d, rsrc_t r, u32 v, u32 s) { __builtin_amdgcn_raw_buffer_store_b64(d, r, v, s, SC1); }
DEVI u32x4 lds128(const LAS char* p) { return *(const LAS u32x4*)p; }
DEVI rsrc_t mkrs(const void* p, u32 n) { return __builtin_amdgcn_make_buffer_rsrc((void*)p, 0, n, 0x00020000); }
constexpr int lastk(int i) { return i < 9 ? 0 : i < 18 ? 1 + (i - 9) / 3 : 1 + (i % 6) / 2; }
constexpr int lastt(int i) { return i < 9 ? i : i < 18 ? (i % 3 + 8) % 9 : 2 * (i / 6 - 2) + (i & 1); }
template <class T> DEVI T sel(bool c, T a, T b) { return c ? a : b; }
DEVI int kcol0(int ks, int q) { return ks < 16 ? 72 * (ks >> 1) + 16 * q + 8 * (ks & 1) : 72 * (4 * (ks - 16) + q) + 64; }
DEVI void fin_stats(float s, float q, float& mean, float& rstd) {
  mean = s * (1.f / 576.f);
  float var = fmaxf(q * (1.f / 576.f) - mean * mean, 0.f);
  rstd = __builtin_amdgcn_rsqf(var + 1e-5f);
}
DEVI void stats16(u32x2 a, u32x2 b, float& mean, float& rstd) {
  float s = __uint_as_float(a[0]) + __uint_as_float(b[0]), q = __uint_as_float(a[1]) + __uint_as_float(b[1]);
  fin_stats(x32sum(x16sum(s)), x32sum(x16sum(q)), mean, rstd);
}
DEVI u32x4 pk8(f32x4 a, f32x4 b) { return u32x4{pk(a[0], a[1]), pk(a[2], a[3]), pk(b[0], b[1]), pk(b[2], b[3])}; }
DEVI f32x4 up4(u32 a, u32 b) { return f32x4{lo16(a), hi16(a), lo16(b), hi16(b)}; }
DEVI u32x2 tr16(const LAS char* p) {
  typedef __attribute__((ext_vector_type(4))) short s16x4;
  return __builtin_bit_cast(u32x2, __builtin_amdgcn_ds_read_tr16_b64_v4i16((LAS s16x4*)p));
}
template <int PACE> DEVI void dma3(rsrc_t ws, u32 lds, u32 voff, u32 soff, int n) {
#pragma unroll 1
  for (int k = 0; k < n; ++k) {
    asm volatile("s_mov_b32 m0, %2\n\ts_nop 0\n\tbuffer_load_dwordx4 %0, %1, %3 offen sc1 lds\n\t"
                 "buffer_load_dwordx4 %0, %1, %3 offen offset:1024 sc1 lds\n\tbuffer_load_dwordx4 %0, %1, %3 offen offset:2048 sc1 lds"
                 :: "v"(voff), "s"(ws), "s"(lds), "s"(soff) : "memory");
    if (PACE) __builtin_amdgcn_s_sleep(PACE);
    lds += 3072; soff += 3072;
  }
}
DEVI void l2pull(rsrc_t r, u32 voff, u32 soff, int n) {
#pragma unroll 1
  for (int k = 0; k < n; ++k) {
    asm volatile("s_mov_b32 m0, %2\n\ts_nop 0\n\tbuffer_load_dword %0, %1, %3 offen sc1 lds" :: "v"(voff), "s"(r), "s"(L_DUMP), "s"(soff) : "memory");
    soff += 8192;
  }
}
DEVI u32x4 ldfrag(rsrc_t r, u32 v, u32 s, int byte) { return ld128(r, v + (byte & 4095), s + (byte & ~4095)); }
DEVI void poll(rsrc_t ws, u32 voff, u32 soff, bool active, u32x2 tag) {
  for (int spin = 0; spin < (1 << 17); ++spin) {
    u32x2 v = ld64(ws, voff, soff);
    asm volatile("" ::: "memory");
    if (__all(!active || (v[0] == tag[0] && v[1] == tag[1]))) break;
  }
  asm volatile("" ::: "memory");
}
DEVI u32x2 next_tag(u32x2 n) {
  u64 v = (((u64)n[1] << 32) | n[0]) * 6364136223846793005ull + 1442695040888963407ull;
  return u32x2{(u32)v, (u32)(v >> 32)};
}


__global__ void __launch_bounds__(512, 2)
fused_fwd(const u16* __restrict__ xin, const u16* __restrict__ wqkv, const u16* __restrict__ wo, const u16* __restrict__ w1,
          const u16* __restrict__ w2, u16* __restrict__ out, char* wsp) {
  extern __shared__ __attribute__((aligned(16))) char smem[];
  const int b = blockIdx.x, xcd = b & 7, rr_ = b >> 3;
  const int m = (xcd >> 2) * 8 + (rr_ >> 2), qd = xcd & 3, r = rr_ & 3, s = 4 * qd + r, cu = m * 16 + s;
  const int th = qd & 1, ch = qd >> 1, c8 = 2 * r + ch;
  const int wave = __builtin_amdgcn_readfirstlane(threadIdx.x >> 6);
  const bool first = (m & 7) == 0;
  int l, t16, g4; u32 l16; bool mact;
  auto relane = [&]() {
    u32 z = 0; OPQ(z);
    l = __builtin_amdgcn_mbcnt_hi(~0u, __builtin_amdgcn_mbcnt_lo(~0u, z)); t16 = l & 15; g4 = l >> 4; l16 = l * 16; mact = (g4 >> 1) == ch;
  };
  relane();
  const rsrc_t ws = mkrs(wsp, WS_END);
  const u32 wt = OFF_WT + s * WT_PER;
  LAS char* const lds = (LAS char*)smem;

  auto attention = [&](int L) {
    const int n = 16 + 16 * wave + t16, head = g4 >> 1, half = g4 & 1;
    const LAS char* kp = lds + L_KS + g4 * 2304 + (n - 7) * 16;
    const LAS char* rp = lds + L_RS + (n - 7) * 8 + head * 4;
    const u32x4 qc = lds128(lds + L_QS + g4 * 2048 + (n - 16) * 16);
    u32 qr = *(const LAS u32*)(rp + 2 * 1152 + 56);
    qr = half ? 0u : qr;
    float sc[8];
#pragma unroll
    for (int w = 0; w < 8; ++w) {
      const u32x4 kc = lds128(kp + (7 - w) * 16);
      const u32 kr = *(const LAS u32*)(rp + (7 - w) * 8);
      sc[w] = dot2(kc[3], qc[3], dot2(kc[2], qc[2], dot2(kc[1], qc[1], dot2(kc[0], qc[0], dot2(kr, qr, 0.f)))));
    }
    float p[4];
#pragma unroll
    for (int w = 0; w < 4; ++w) {
      auto sw = __builtin_amdgcn_permlane16_swap(__float_as_uint(sc[w]), __float_as_uint(sc[w + 4]), false, false);
      p[w] = __uint_as_float(sw[0]) + __uint_as_float(sw[1]);
      if (first && n - 16 - (w + 4 * half) < 0) p[w] = -1e30f;
    }
    float mx = fmaxf(fmaxf(p[0], p[1]), fmaxf(p[2], p[3]));
    { auto sw = __builtin_amdgcn_permlane16_swap(__float_as_uint(mx), __float_as_uint(mx), false, false); mx = fmaxf(__uint_as_float(sw[0]), __uint_as_float(sw[1])); }
#pragma unroll
    for (int w = 0; w < 4; ++w) p[w] = __builtin_amdgcn_exp2f(p[w] - mx);
    const float inv = __builtin_amdgcn_rcpf(x16sum((p[0] + p[1]) + (p[2] + p[3])));
    const u32 pa = pk(p[0] * inv, p[1] * inv), pb = pk(p[2] * inv, p[3] * inv);
    auto sa = __builtin_amdgcn_permlane16_swap(pa, pa, false, false);
    auto sb = __builtin_amdgcn_permlane16_swap(pb, pb, false, false);
    const u32 P[4] = {sa[0], sb[0], sa[1], sb[1]};
    float o[8] = {0, 0, 0, 0, 0, 0, 0, 0}, orest = 0.f;
    const u32 sel = half ? 0x07060302u : 0x05040100u;
#pragma unroll
    for (int i = 0; i < 4; ++i) {
      const u32x4 va = lds128(kp + 9216 + (7 - 2 * i) * 16), vb = lds128(kp + 9216 + (6 - 2 * i) * 16);
      const u32 ra = *(const LAS u32*)(rp + 1152 + (7 - 2 * i) * 8), rb = *(const LAS u32*)(rp + 1152 + (6 - 2 * i) * 8);
#pragma unroll
      for (int d = 0; d < 4; ++d) { o[2 * d] = dot2(plo(va[d], vb[d]), P[i], o[2 * d]); o[2 * d + 1] = dot2(phi(va[d], vb[d]), P[i], o[2 * d + 1]); }
      orest = dot2(__builtin_amdgcn_perm(rb, ra, sel), P[i], orest);
    }
    const u32 ob = OFF_OB + L * XB_SZ + (m * 8 + wave) * 18432;
    st128(u32x4{pk(o[0], o[1]), pk(o[2], o[3]), pk(o[4], o[5]), pk(o[6], o[7])}, ws, l16, ob + s * 1024);
    auto s1 = __builtin_amdgcn_permlane16_swap(__float_as_uint(orest), __float_as_uint(orest), false, false);
    const u32 dr = pk(__uint_as_float(s1[0]), __uint_as_float(s1[1]));
    auto s2 = __builtin_amdgcn_permlane32_swap(dr, dr, false, false);
    st64(u32x2{s2[0], s2[1]}, ws, l < 16 ? l * 8 : OOB, ob + 16384 + s * 128);
    WAITV(0);
  };

  auto pull_code = [&]() {
    u64 pc; int len;
    asm volatile("s_getpc_b64 %0\n1:\n\ts_mov_b32 %1, .Lfwd_end-1b" : "=s"(pc), "=s"(len));
    l2pull(mkrs((const void*)pc, len > 0 ? len : 0), l < 14 ? l * 128 : OOB, rr_ * 1792, 1);
  };
  asm volatile("" :: "s"(xin), "s"(wqkv), "s"(wo), "s"(wsp));
  if (wave >= 4) {
    const int un = (wave - 4) * 16 + m;
    const bool k1 = un >= 18;
    const int ks = un - sel(k1, 18, 0);
    const bool lo = ks < 16;
    const u32 pitch = sel(k1, 1152, 3456);
    const rsrc_t sr = mkrs(sel(k1, wo, wqkv), pitch * 576);
    const int r0 = sel(lo, 36 * ks - sel(k1, 0, 28 * (ks & 1)), 288 * ks - 4608 + sel(k1, 16, 64));
    const u32 A = sel(k1 && !lo, 36, 4) * pitch, B = sel(lo, sel(k1, 8, 16), 72) * pitch, C = sel(lo, sel(k1, 20, 32), 144) * pitch;
    const u32 base = r0 * pitch + sel(k1, 144 * c8, 72 * s - 8 * (s & 1));
    const int seg = (l * sel(k1, 57, 103)) >> 9;
    const u32 voff = seg * sel(k1, 1152 - 144, 1152 - 80) + l16, step = sel(k1, 576, 960);
    if (l < sel(un < 36, sel(k1, 36, 60), 0)) {
      u32 dst = (wave - 4) * 9216;
#pragma unroll
      for (int i = 0; i < 8; ++i) {
        asm volatile("s_mov_b32 m0, %2\n\ts_nop 0\n\tbuffer_load_dwordx4 %0, %1, %3 offen lds" :: "v"(voff), "s"(sr), "s"(dst), "s"(base + (i & 1 ? A : 0) + (i & 2 ? B : 0) + (i & 4 ? C : 0)) : "memory");
        dst += step;
      }
    }
  }
  if (wave == 7) pull_code();
  u32x4 rc0, rc1; u32x2 rcm;
  auto rowoff = [&]() { return (u32)(2 * (128 * (m & 7) + 64 * th + 16 * wave + t16) + (m >> 3)) * 1152 + 144 * c8; };
  if (wave < 4 && l < 54) {
    const rsrc_t xr = mkrs(xin, XB_SZ);
    const int row = (l * 57) >> 9;
    const u32 vo = row * 2304 + (l - 9 * row) * 16, so = (2 * (128 * (m & 7) + 64 * th + 16 * wave) + (m >> 3)) * 1152 + 144 * c8;
#pragma unroll
    for (int i = 0; i < 3; ++i)
      asm volatile("s_mov_b32 m0, %2\n\ts_nop 0\n\tbuffer_load_dwordx4 %0, %1, %3 offen lds" :: "v"(i == 2 && l >= 36 ? OOB : vo), "s"(xr), "s"(SLOT + wave * 4096 + i * 864), "s"(so + i * 13824) : "memory");
  }
  int role = wave; OPQ(role);
  if (__builtin_expect(role < 4, 0)) {
    const int w = wave;
    auto unpack_res = [&](f32x4* res) {
      res[0] = up4(rc0[0], rc0[1]); res[1] = up4(rc0[2], rc0[3]); res[2] = up4(rc1[0], rc1[1]); res[3] = up4(rc1[2], rc1[3]); res[4] = up4(rcm[0], rcm[1]);
    };
    auto pack_res = [&](const f32x4* res) { rc0 = pk8(res[0], res[1]); rc1 = pk8(res[2], res[3]); rcm = u32x2{pk(res[4][0], res[4][1]), pk(res[4][2], res[4][3])}; };
    auto publish_x = [&](int xi) {
      const u32 xb = OFF_XB + xi * XB_SZ + (m * 8 + 4 * th + w) * 18432;
      st128(rc0, ws, l16, xb + 2 * c8 * 1024);
      st128(rc1, ws, l16, xb + (2 * c8 + 1) * 1024);
      st64(rcm, ws, mact ? t16 * 16 + (g4 & 1) * 8 : OOB, xb + (16 + (c8 >> 2)) * 1024 + (c8 & 3) * 256);
      const u32 m0 = mact ? rcm[0] : 0, m1 = mact ? rcm[1] : 0;
      float s1 = dot2(m0, ONES, 0.f), s2 = dot2(m0, m0, 0.f), u1 = dot2(m1, ONES, 0.f), u2 = dot2(m1, m1, 0.f);
#pragma unroll
      for (int j = 0; j < 4; ++j) { s1 = dot2(rc0[j], ONES, s1); s2 = dot2(rc0[j], rc0[j], s2); u1 = dot2(rc1[j], ONES, u1); u2 = dot2(rc1[j], rc1[j], u2); }
      s1 += u1; s2 += u2;
      s1 = x32sum(x16sum(s1)); s2 = x32sum(x16sum(s2));
      st64(u32x2{__float_as_uint(s1), __float_as_uint(s2)}, ws, l < 16 ? l * 8 : OOB, OFF_ST + xi * ST_SZ + ((m * 8 + c8) * 128 + 64 * th + 16 * w) * 8);
    };
    WAITV(0);
    { const LAS char* xp = lds + SLOT + w * 4096 + t16 * 144; rc0 = lds128(xp + 32 * g4); rc1 = lds128(xp + 32 * g4 + 16); rcm = *(const LAS u32x2*)(xp + 128 + 8 * (g4 & 1)); }
    publish_x(0);
    PIN(rc0); PIN(rc1); PIN(rcm);
    WAITV(0);
    BAR();
    for (int L = 0; L < 2; ++L) {
      BAR();
      relane();
      {
        const u32 sb = OFF_ST + 2 * L * ST_SZ + (m * 8 * 128 + 32 * w) * 8, sv = g4 * 2048 + t16 * 8;
        u32x2 st[4];
        const u32 xb = OFF_XB + 2 * L * XB_SZ + (m * 8 + 2 * w) * 18432;
        const u32 l16b = l16 + 18432;
        u32x4 xq[18][2];
#pragma unroll
        for (int ks = 0; ks < QW; ++ks) { xq[ks][0] = ldfrag(ws, l16, xb, (ks) * 1024); xq[ks][1] = ldfrag(ws, l16b, xb, (ks) * 1024); }
        if (L == 0) BAR();
        f32x4 acc[7][2], cs[7];
        u32x4 a[8];
        float mean[2], rstd[2], sc[2][3], nm[2][3], tm[7][2][4];
        u32 aK, aQ, aR;
        int o1 = 64512; OPQ(o1);
        const LAS char* w0 = lds + l16; const LAS char* w1p = w0 + o1;
#pragma unroll
        for (int i = 0; i < 7; ++i) a[i] = lds128(w0 + i * 1024);
        SB();
        sfor<126>([&](auto ic) __attribute__((always_inline)) {
          constexpr int i = ic, ks = i / 7, T = i % 7;
          if (i % 3 == 0) USE(a[(i + 2) & 7]);
          if (T == 0) USE(xq[ks][1]);
          mac16(acc[T][0], a[i & 7], xq[ks][0], ks == 0);
          SB();
          if (i + 7 < 126) a[(i + 7) & 7] = i + 7 < 63 ? lds128(w0 + (i + 7) * 1024) : lds128(w1p + (i - 56) * 1024);
          SB();
          if constexpr (ks == 13 && T < 2) {
            sc[T][0] = rstd[T]; sc[T][1] = rstd[T] * (0.23570226039551584f * LOG2E); sc[T][2] = g4 == 2 ? sc[T][1] : rstd[T];
            PIN(sc[T][1]); PIN(sc[T][2]);
          }
          if constexpr (ks == 13 && T >= 2 && T < 5) { nm[0][T - 2] = -mean[0] * sc[0][T - 2]; nm[1][T - 2] = -mean[1] * sc[1][T - 2]; PIN(nm[0][T - 2]); PIN(nm[1][T - 2]); }
          if constexpr (ks == 13 && T == 5) { aK = L_KS + g4 * 2304 + (16 + 32 * w + t16) * 16; aQ = L_QS + g4 * 2048 + (32 * w + t16) * 16; PIN(aK); PIN(aQ); }
          if constexpr (ks == 13 && T == 6) { aR = sel<u32>(g4 < 3, L_RS + g4 * 1152 + (16 + 32 * w) * 8, L_DUMP) + t16 * 8; PIN(aR); }
          if constexpr (ks >= 14) {
            constexpr int u = (i - 98) >> 2, j = (i - 98) & 3, q = u < 4 ? 0 : u < 6 ? 1 : 2;
            asm volatile("v_mul_f32 %0, %2, %3\n\tv_mul_f32 %1, %2, %4" : "=&v"(tm[u][0][j]), "=v"(tm[u][1][j]) : "v"(cs[u][j]), "v"(nm[0][q]), "v"(nm[1][q]));
          }
          SB();
          mac16(acc[T][1], a[i & 7], xq[ks][1], ks == 0);
          SB();
          if constexpr (ks + QW < 18 && T == 1) xq[ks + QW][0] = ldfrag(ws, l16, xb, (ks + QW) * 1024);
          if constexpr (ks + QW < 18 && T == 4) xq[ks + QW][1] = ldfrag(ws, l16b, xb, (ks + QW) * 1024);
          if constexpr (ks >= 2 && ks < 6 && T == 6) st[ks - 2] = ld64(ws, sv + ((ks - 2) >> 1) * 128 + (ks & 1) * 1024, sb);
          if constexpr ((ks == 4 || ks == 10) && T == 6) { if (L == 0) BAR(); }
          if constexpr (ks == 12 && T == 6) {
            stats16(st[0], st[1], mean[0], rstd[0]); stats16(st[2], st[3], mean[1], rstd[1]);
            PIN(mean[0]); PIN(mean[1]); PIN(rstd[0]); PIN(rstd[1]);
            BAR();
          }
          if (ks == 13) cs[T] = *(const LAS f32x4*)(lds + L_CSQ + T * 64 + g4 * 16);
          SB();
        });
        MFMA_DONE();
#pragma unroll
        for (int T = 0; T < 7; ++T) { PIN(acc[T][0]); PIN(acc[T][1]); }
#pragma unroll
        for (int e = 0; e < 2; ++e) {
          f32x4 v[7];
#pragma unroll
          for (int T = 0; T < 7; ++T) v[T] = acc[T][e] * sc[e][T < 4 ? 0 : T < 6 ? 1 : 2] + f32x4{tm[T][e][0], tm[T][e][1], tm[T][e][2], tm[T][e][3]};
          *(LAS u32x4*)(lds + aK + e * 256) = pk8(v[0], v[1]);
          *(LAS u32x4*)(lds + aK + e * 256 + (L_VS - L_KS)) = pk8(v[2], v[3]);
          *(LAS u32x4*)(lds + aQ + e * 256) = pk8(v[4], v[5]);
          *(LAS u32x2*)(lds + aR + e * 128) = u32x2{pk(v[6][0], v[6][1]), pk(v[6][2], v[6][3])};
        }
        relane();
        WAITL();
        BAR();
      }
      attention(L);
      BAR();
      BAR();
      relane();
      {
        const u32 ob = OFF_OB + L * XB_SZ + (m * 8 + 4 * th + w) * 18432;
        u32x4 of[18];
#pragma unroll
        for (int ks = 0; ks < 16; ++ks) of[ks] = ld128(ws, l16 + (ks & 3) * 1024, ob + (ks >> 2) * 4096);
#pragma unroll
        for (int k = 0; k < 2; ++k) {
          const u32x2 lo = ld64(ws, g4 * 256 + t16 * 8 + k * 1024, ob + 16384), hi = ld64(ws, g4 * 256 + t16 * 8 + k * 1024 + 128, ob + 16384);
          of[16 + k] = u32x4{lo[0], lo[1], hi[0], hi[1]};
        }
        u32x4 a[10];
        f32x4 res[5];
        unpack_res(res);
        int o0 = SLOT, o1 = SLOT + 46080; OPQ(o0); OPQ(o1);
        const LAS char* w0 = lds + l16 + o0; const LAS char* w1p = lds + l16 + o1;
#pragma unroll
        for (int i = 0; i < 10; ++i) a[i] = lds128(w0 + i * 1024);
#pragma unroll
        for (int T = 0; T < 5; ++T) PIN(res[T]);
        asm volatile("s_nop 1");
        SB();
#pragma unroll
        for (int i = 0; i < 90; ++i) {
          if (i % 3 == 0) USE(a[(i + 2) % 10]);
          mac16(res[i % 5], a[i % 10], of[i / 5], false);
          SB();
          if (i + 10 < 90) a[i % 10] = i + 10 < 45 ? lds128(w0 + (i + 10) * 1024) : lds128(w1p + (i - 35) * 1024);
          SB();
        }
        MFMA_DONE();
#pragma unroll
        for (int T = 0; T < 5; ++T) PIN(res[T]);
        pack_res(res);
        PIN(rc0); PIN(rc1); PIN(rcm);
        publish_x(2 * L + 1);
        PIN(rc0); PIN(rc1); PIN(rcm);
        WAITV(0);
      }
      BAR();
      BAR();
      relane();
      float rstd_own;
      {
        const u32 sb = OFF_ST + (2 * L + 1) * ST_SZ + m * 8 * 128 * 8, sv = g4 * 2048 + t16 * 8;
        u32x2 st[6];
#pragma unroll
        for (int i = 0; i < 4; ++i) st[i] = ld64(ws, sv + (i >> 1) * 128 + (i & 1) * 1024, sb + 32 * w * 8);
#pragma unroll
        for (int i = 0; i < 2; ++i) st[4 + i] = ld64(ws, sv + i * 1024, sb + (64 * th + 16 * w) * 8);
        const u32 xb = OFF_XB + (2 * L + 1) * XB_SZ + (m * 8 + 2 * w) * 18432, l16b = l16 + 18432;
        u32x4 x[18][2];
#pragma unroll
        for (int ks = 0; ks < FW; ++ks) { x[ks][0] = ldfrag(ws, l16, xb, (ks) * 1024); x[ks][1] = ldfrag(ws, l16b, xb, (ks) * 1024); }
        u32x4 a[9];
        const LAS char* lb = lds + l16;
        int so = 0, sn = SLOT;
        OPQ(so); OPQ(sn);
        auto dual = [&](int i) { return lds128(lb + ((i & 2) ? sn : so) + ((i >> 2) * 2 + (i & 1)) * 1024); };
#pragma unroll
        for (int i = 0; i < 5; ++i) a[i] = dual(i);
        float c2[2];
        u32x2 bm[2], acs[2];
#pragma unroll
        for (int e = 0; e < 2; ++e) {
          float mean, rstd;
          stats16(st[2 * e], st[2 * e + 1], mean, rstd);
          c2[e] = -LOG2E * rstd;
          const u32 mh = pk(-mean, -mean); const float ml = -mean - hi16(mh);
          bm[e] = u32x2{g4 == 0 ? mh : g4 == 1 ? pk(ml, ml) : 0u, 0u};
          acs[e][1] = 0u;
        }
        { float mo; stats16(st[4], st[5], mo, rstd_own); }
        const LAS char* csp = lds + L_CS1 + t16 * 4;
        auto next_cs = [&]() { acs[0][0] = *(const LAS u32*)csp; asm volatile("" ::: "memory"); SB(); acs[1][0] = *(const LAS u32*)(csp + 64); csp += 128; };
        auto fold = [&](f32x4* cur) { mul16x4(cur, acs, bm); SB(); };
        u32 hoff = OFF_HB + L * HB_SZ + cu * 73728 + w * 2048;
        u32 hs[4];
#pragma unroll
        for (int k = 0; k < 4; ++k) hs[k] = OFF_HB + L * HB_SZ + (m * 16 + 4 * qd + k) * 73728 + w * 2048;
        f32x4 acc0[4], acc1[4];
        u32x4 hb[2][8];
        f32x4 c[9][2];
        u32x4 h0, h1; float te, ty[2], tm, ta, tr;
        auto silu_gap = [&](int t, f32x4* prev) {
          const int e = t >> 2, q = t & 3;
          SB();
          if (q == 0) {
            if (e < 16) asm volatile("v_mul_f32 %0, %1, %2" : "=v"(tm) : "v"(prev[e >> 2][e & 3]), "v"(c2[e >> 3]));
            if (e >= 3 && (e & 1)) { const int f = (e - 3) >> 1; u32 p; asm volatile("v_cvt_pk_bf16_f32 %0, %1, %2" : "=v"(p) : "v"(ty[0]), "v"(ty[1])); if (f < 4) h0[f] = p; else h1[f - 4] = p; }
          }
          if (q == 1 && e < 16) asm volatile("v_exp_f32 %0, %1" : "=v"(te) : "v"(tm));
          if (q == 2) {
            if (e < 16) asm volatile("v_add_f32 %0, 1.0, %1" : "=v"(ta) : "v"(te));
            if (e >= 1 && e < 17) asm volatile("v_mul_f32 %0, %1, %2" : "=v"(ty[(e - 1) & 1]) : "v"(prev[(e - 1) >> 2][(e - 1) & 3]), "v"(tr));
          }
          if (q == 3 && e < 16) asm volatile("v_rcp_f32 %0, %1" : "=v"(tr) : "v"(ta));
          if (t == 65) st128(h0, ws, l16, hoff);
          if (t == 69) { st128(h1, ws, l16 + 1024, hoff); hoff += 8192; }
        };
        auto advance = [&]() { so = sn; sn = sn == 3 * SLOT ? 0 : sn + SLOT; asm volatile("" : "+s"(so), "+s"(sn)); };
        auto f1step = [&](auto last, f32x4* cur, f32x4* prev) __attribute__((always_inline)) {
          constexpr bool LAST = last;
          const LAS char* base = lb + so; const LAS char* nbase = lb + sn;
          fold(cur);
          if (!LAST) next_cs();
          SB();
          sfor<36>([&](auto ic) __attribute__((always_inline)) {
            constexpr int i = ic;
            mac16(cur[i & 1], a[i % 6], x[i >> 1][0], false);
            SB();
            a[(i + 5) % 6] = i + 5 < 36 ? lds128(base + (i + 5) * 1024) : lds128(nbase + (i - 31) * 1024);
            silu_gap(2 * i, prev);
            SB();
            mac16(cur[2 + (i & 1)], a[i % 6], x[i >> 1][1], false);
            SB();
            USE(a[(i + 1) % 6]);
            silu_gap(2 * i + 1, prev);
            if constexpr (LAST && i >= 3 && i < 35 && (i & 1)) { constexpr int k = (i - 3) >> 1; hb[k >> 3][k & 7] = ld128(ws, l16 + (k & 1) * 1024, hs[(k & 7) >> 1] + (k >> 3) * 8192); }
            if constexpr (i == 30) { if (!LAST) WAITV(0); BAR(); }
            SB();
          });
          advance();
        };
        auto f2step = [&](auto first_, auto par, u32 hv, u32 hnext) __attribute__((always_inline)) {
          constexpr bool FIRST = first_; constexpr int P = par;
          const LAS char* base = lb + so; const LAS char* nbase = lb + sn;
          if (FIRST) { a[5] = lds128(base + 5120); a[6] = lds128(base + 6144); a[7] = lds128(base + 7168); }
          sfor<36>([&](auto ic) __attribute__((always_inline)) {
            constexpr int i = ic, kk = i / 9, T = i % 9;
            if (T % 3 == 0) USE(a[(T + 2) % 9]);
            if (T == 0) USE(hb[P][2 * kk + 1]);
            mac16(c[T][0], a[T], hb[P][2 * kk], FIRST && kk == 0);
            if (FIRST && i == 0) asm volatile("s_nop 7");
            SB();
            a[(i + 8) % 9] = i + 8 < 36 ? lds128(base + (i + 8) * 1024) : lds128(nbase + (i - 28) * 1024);
            if (FIRST) silu_gap(2 * i, acc0);
            SB();
            mac16(c[T][1], a[T], hb[P][2 * kk + 1], FIRST && kk == 0);
            SB();
            if (FIRST) { if (T % 3 == 2) USE(a[(T + 3) % 9]); silu_gap(2 * i + 1, acc0); }
            if constexpr (T == 8) hb[P][2 * kk] = ld128(ws, hv, hs[kk] + hnext);
            if constexpr (T == 3 && kk > 0) hb[P][2 * kk - 1] = ld128(ws, hv + 1024, hs[kk - 1] + hnext);
            if constexpr (i == 35) hb[P][7] = ld128(ws, hv + 1024, hs[3] + hnext);
            if constexpr (i == 27) { if (P == 1) WAITV(7); BAR(); }
            SB();
          });
          advance();
        };
        next_cs();
        PIN(rstd_own); PIN(bm[0]); PIN(bm[1]); PIN(acs[0]); PIN(acs[1]);
        asm volatile("s_nop 1");
        SB();
        fold(acc0);
        next_cs();
        SB();
        fold(acc1);
        next_cs();
        SB();
        sfor<72>([&](auto ic) __attribute__((always_inline)) {
          constexpr int i = ic, ks = i >> 2;
          f32x4* cur = (i & 2) ? acc1 : acc0;
          if ((i & 1) == 0) USE(a[(i + 1) % 6]);
          mac16(cur[i & 1], a[i % 6], x[ks][0], false);
          SB();
          if constexpr (i == 67) { advance(); advance(); }
          a[(i + 5) % 6] = i + 5 < 72 ? dual(i + 5) : lds128(lb + so + (i - 67) * 1024);
          SB();
          mac16(cur[2 + (i & 1)], a[i % 6], x[ks][1], false);
          SB();
          if constexpr (ks + FW < 18 && (i & 1)) x[ks + FW][(i >> 1) & 1] = ldfrag(ws, (i & 2) ? l16b : l16, xb, (ks + FW) * 1024);
          if constexpr (i == 66) BAR();
          SB();
        });
        asm volatile("s_nop 7");
#pragma unroll
        for (int e = 0; e < 2; ++e) {
          PIN(acc0[2 * e]); PIN(acc0[2 * e + 1]);
          f32x4 y[2];
#pragma unroll
          for (int j = 0; j < 8; ++j) { const float v = acc0[2 * e + (j >> 2)][j & 3]; y[j >> 2][j & 3] = v * __builtin_amdgcn_rcpf(1.f + __builtin_amdgcn_exp2f(v * c2[e])); }
          st128(pk8(y[0], y[1]), ws, l16 + e * 1024, hoff);
        }
        hoff += 8192;
        BAR();
#pragma unroll 1
        for (int j = 0; j < 3; ++j) { f1step(IC<0>{}, acc0, acc1); f1step(IC<0>{}, acc1, acc0); }
        f1step(IC<1>{}, acc0, acc1);
        f2step(IC<1>{}, IC<0>{}, l16, 2 * 8192);
#pragma unroll 1
        for (int j = 0; j < 3; ++j) { f2step(IC<0>{}, IC<1>{}, l16, (3 + 2 * j) * 8192); f2step(IC<0>{}, IC<0>{}, l16, (4 + 2 * j) * 8192); }
        f2step(IC<0>{}, IC<1>{}, OOB, 0);
        const u32 pb = OFF_PB + L * PB_SZ + (((m * 4 + r) * 4 + qd) * 4 + w) * 9216;
        auto put = [&](int p, int e) __attribute__((always_inline)) {
          PIN(c[2 * p][e]); PIN(c[2 * p + 1][e]);
          const u32x4 k0 = pk8(c[2 * p][e], c[2 * p + 1][e]);
          if ((w >> 1) == th && (p >> 1) == ch) *(LAS u32x4*)(lds + L_OWN + ((w & 1) * 2 + e) * 2048 + (p & 1) * 1024 + l16) = k0;
          else st128(k0, ws, l16 + p * 1024, pb + e * 4096);
        };
        sfor<36>([&](auto ic) __attribute__((always_inline)) {
          constexpr int i = ic, kk = lastk(i), T = lastt(i);
          if (i % 3 == 0) USE(a[(i + 2) % 9]);
          if (i == 0) USE(hb[0][1]);
          if (i == 9) USE(hb[0][7]);
          mac16(c[T][0], a[i % 9], hb[0][2 * kk], false);
          SB();
          if (i + 8 < 36) a[(i + 8) % 9] = lds128(lb + so + (lastk(i + 8) * 9 + lastt(i + 8)) * 1024);
          SB();
          mac16(c[T][1], a[i % 9], hb[0][2 * kk + 1], false);
          SB();
          if constexpr (i == 17) { PIN(c[8][0]); PIN(c[8][1]); st128(pk8(c[8][0], c[8][1]), ws, l16, pb + 8192); }
          if constexpr (i >= 19 && (i % 6 == 1 || i % 6 == 3)) put(i / 6 - 3, i % 6 == 3);
          if constexpr (i == 27) BAR();
          SB();
        });
        MFMA_DONE();
        put(3, 0); put(3, 1);
        WAITL();
        WAITV(0);
      }
      BAR();
      {
        relane();
        const u32 pb = OFF_PB + L * PB_SZ + ((m * 4 + r) * 16 + 2 * th + (w >> 1)) * 9216;
        u32 vm = mact ? l16 : OOB;
        u32x4 pc[4][2]; u32x2 pm[4];
        pm[0] = ld64(ws, vm, pb + qd * 36864 + 8192 + (w & 1) * 8);
        pc[0][0] = lds128(lds + L_OWN + w * 2048 + l16);
        pc[0][1] = lds128(lds + L_OWN + w * 2048 + 1024 + l16);
        f32x4 t[5] = {up4(pc[0][0][0], pc[0][0][1]), up4(pc[0][0][2], pc[0][0][3]), up4(pc[0][1][0], pc[0][1][1]), up4(pc[0][1][2], pc[0][1][3]), up4(pm[0][0], pm[0][1])};
        f32x4 res[5];
        unpack_res(res);
#pragma unroll
        for (int T = 0; T < 5; ++T) { PIN(t[T]); PIN(res[T]); }
        PIN(vm);
        BAR();
#pragma unroll
        for (int k = 1; k < 4; ++k) {
          const u32 pk_ = pb + ((qd + k) & 3) * 36864;
          pc[k][0] = ld128(ws, l16, pk_ + ((w & 1) * 4 + ch * 2) * 1024);
          pc[k][1] = ld128(ws, l16 + 1024, pk_ + ((w & 1) * 4 + ch * 2) * 1024);
          pm[k] = ld64(ws, vm, pk_ + 8192 + (w & 1) * 8);
        }
#pragma unroll
        for (int k = 1; k < 4; ++k) {
          t[0] += up4(pc[k][0][0], pc[k][0][1]); t[1] += up4(pc[k][0][2], pc[k][0][3]);
          t[2] += up4(pc[k][1][0], pc[k][1][1]); t[3] += up4(pc[k][1][2], pc[k][1][3]); t[4] += up4(pm[k][0], pm[k][1]);
        }
#pragma unroll
        for (int T = 0; T < 5; ++T) res[T] += rstd_own * t[T];
        pack_res(res);
      }
      if (L == 0) {
        publish_x(2);
        PIN(rc0); PIN(rc1); PIN(rcm);
        WAITV(0);
        BAR();
      }
    }
    const rsrc_t orr = mkrs(out, XB_SZ);
    relane();
    __builtin_amdgcn_raw_buffer_store_b128(rc0, orr, rowoff() + 32 * g4, 0, 0);
    __builtin_amdgcn_raw_buffer_store_b128(rc1, orr, rowoff() + 32 * g4 + 16, 0, 0);
    __builtin_amdgcn_raw_buffer_store_b64(rcm, orr, mact ? rowoff() + 128 + 8 * (g4 & 1) : OOB, 0, 0);
    asm volatile(".Lfwd_end:");
  } else {
    const int hw = wave - 4;
    const int cons = sel(hw == 3, cu, sel(g4 < 2, ((m + g4) & 15) * 16 + t16, t16 * 16 + s));
    const u32 mail = OFF_MAIL + cons * 4096;
    const int un = hw * 16 + m;
    auto unit_load = [&](int kind, int uk, u32 dst) {
      const int ks = kind == 3 ? uk >> 1 : uk;
      const bool lo = ks < 16;
      const u32 pitch = kind == 0 ? 3456 : kind == 3 ? 9216 : 1152;
      const rsrc_t sr = mkrs(kind == 0 ? wqkv : kind == 1 ? wo : kind == 3 ? w1 : w2, pitch * (kind == 4 ? 4608 : 576));
      const int r0 = kind == 0 ? kcol0(ks, 0) : kind == 1 ? (lo ? 36 * ks : 288 * (ks - 16) + 16) : kind == 3 ? kcol0(ks, 0) : 288 * (4 * qd + (uk & 3)) + 32 * (uk >> 2);
      const int dq = lo ? 16 : 72;
      const u32 A = (kind == 0 ? 4 : kind == 1 ? (lo ? 4 : 36) : 2) * pitch, B = (kind == 0 ? dq : kind == 1 ? (lo ? 8 : 72) : 4) * pitch;
      const u32 C = (kind == 0 ? 2 * dq : kind == 1 ? (lo ? 20 : 144) : kind == 3 ? dq : 8) * pitch, Dd = (kind == 3 ? 2 * dq : 16) * pitch;
      const u32 base = r0 * pitch + (kind == 0 ? 72 * s - 8 * (s & 1) : kind == 1 ? 144 * c8 : kind == 3 ? 576 * s + 288 * (uk & 1) : 288 * r);
      const int gpr = kind == 0 ? 5 : kind == 1 ? 9 : 18;
      const int seg = (l * (kind == 0 ? 103 : kind == 1 ? 57 : 29)) >> 9;
      const u32 voff = seg * (kind == 3 ? 9216 : 1152) + (l - seg * gpr) * 16;
      const u32 step = kind == 0 ? 960 : 576;
      if (l < (kind == 0 ? 60 : 36)) {
#pragma unroll 1
        for (int j = 0; j < (kind >= 3 ? 4 : 2); ++j) {
          const u32 sj = base + (j & 1 ? C : 0) + (j & 2 ? Dd : 0);
#pragma unroll
          for (int i = 0; i < 4; ++i) {
            asm volatile("s_mov_b32 m0, %2\n\ts_nop 0\n\tbuffer_load_dwordx4 %0, %1, %3 offen lds" :: "v"(voff), "s"(sr), "s"(dst), "s"(sj + (i & 1 ? A : 0) + (i & 2 ? B : 0)) : "memory");
            dst += step;
          }
        }
      }
    };
    auto unit_read = [&](int kind, u32 stg, u32x4* f, bool pre = false) {
      const int qq = t16 >> 2, p = t16 & 3;
      const int row = sel(kind == 4, 4 * g4 + qq, 8 * g4 + qq) * sel(kind == 0, 240, sel(kind == 1, 144, 288)) + sel(kind == 0, 8 * (s & 1), 0);
      const int a0 = sel(kind == 0, 80 + 40 * (p >> 1) + 16 * (p & 1), sel(kind == 3, 8 * p, 32 * p));
      const int aM = sel(kind == 0, sel(p == 2, 32, 112 + 80 * (p & 1)), sel(kind == 3, a0 + 256, 128 + 8 * (p & 1) + sel(kind == 4, 144 * (p >> 1), 0)));
      const int E = kind == 3 ? 32 : 8, Fo = sel(kind == 0, 80, sel(kind == 3, 64, 16)), G = sel(kind == 0, -80, sel(kind == 3, 128, 144));
      const int rs = sel(kind == 0, 960, sel(kind == 1, 576, sel(kind == 3, 1152, 4608)));
      u32 ad[10];
#pragma unroll
      for (int T = 0; T < 5; ++T) { ad[T] = stg + row + (T == 4 ? aM : a0 + (T & 1 ? Fo : 0) + (T & 2 ? G : 0)); ad[5 + T] = ad[T] + rs; }
      if (pre) {
#pragma unroll
        for (int T = 0; T < 10; ++T) PIN(ad[T]);
        WAITV(0);
      }
#pragma unroll
      for (int T = 0; T < 9; ++T) {
        const u32x2 x = tr16(lds + ad[T >> 1] + (T & 1 ? E : 0)), y = tr16(lds + ad[5 + (T >> 1)] + (T & 1 ? E : 0));
        f[T] = u32x4{x[0], x[1], y[0], y[1]};
      }
    };
    auto unit_store = [&](int kind, int uk, const u32x4* f) {
      const int nm = kind == 0 ? 6 : kind == 1 ? 4 : 8;
      u32 dst = wt + sel<u32>(kind == 0, uk * 7168, sel<u32>(kind == 1, WT_O + uk * 5120, sel<u32>(kind == 3, WT_F + (uk >> 1) * 2048, WT_F + (324 + uk * 9) * 1024)));
      OPQ(dst);
#pragma unroll
      for (int T = 0; T < 9; ++T) {
        const int tg = 9 * (uk & 1) + T;
        if (T < 4 || T == 8 || T < nm) st128(f[T], ws, l16, dst + (kind == 3 ? (tg >> 1) * SLOT + (tg & 1) * 1024 : (T == 8 ? nm : T) * 1024));
      }
      if (kind == 3) {
        float cs[9];
#pragma unroll
        for (int T = 0; T < 9; ++T) cs[T] = x32sum(x16sum(dot2(f[T][3], ONES, dot2(f[T][2], ONES, dot2(f[T][1], ONES, dot2(f[T][0], ONES, 0.f))))));
#pragma unroll
        for (int k = 0; k < 3; ++k) {
          const float v = k == 2 ? cs[8] : g4 == 0 ? cs[4 * k] : g4 == 1 ? cs[4 * k + 1] : g4 == 2 ? cs[4 * k + 2] : cs[4 * k + 3];
          __builtin_amdgcn_raw_buffer_store_b32(__float_as_uint(v), ws, k < 2 || l < 16 ? l * 4 + k * 256 : OOB, OFF_CS + s * CS_PER + uk * 576, SC1);
        }
      }
    };
    const u32x2 nonce = ld64(ws, hw >= 2 ? cons * 128 : OOB, OFF_NONCE);
    u32x2 tag = {0, 0};
    if (hw == 3) l2pull(ws, l < 32 ? l * 128 : OOB, OFF_MAIL + cu * 4096, 1);
    asm volatile("" :: "s"(w1));
    auto pull_unit = [&](int uk) {
      const u32 o = (288 * (4 * qd + (uk & 3)) + 32 * (uk >> 2)) * 1152 + r * 9216;
      l2pull(mkrs(w2, 4608 * 1152), l * 128, o, 1);
      l2pull(mkrs(w2, 4608 * 1152), l < 8 ? l * 128 : OOB, o + 8192, 1);
    };
    for (int L = 0; L < 2; ++L) {
      const u32 ph = L * 2048;
      relane();
      u32 fo = sel<u32>(g4 == 0, mail + s * 8, sel<u32>(g4 == 1, sel<u32>(th == 1 && (m & 7) != 7, mail + (16 + c8) * 8, OOB), sel<u32>(g4 == 2 && L == 0, mail + 256 + m * 8, OOB)));
      PIN(fo);
      if (L == 0) {
        u32x4 f[9];
        unit_read(un >= 18, hw * 9216, f, true);
        tag = nonce; PIN(tag); tag = next_tag(tag); PIN(tag);
        if (un < 36) { unit_store(un >= 18, un - sel(un >= 18, 18, 0), f); unit_load(3, un, L_KS + hw * 9216); }
        WAITV(16);
      }
      BAR();
      if (hw == 2) st64(tag, ws, fo, ph);
      if (hw < 2 && L == 1) dma3<0>(ws, SLOT + hw * 18432, l16, wt + SLOT + hw * 18432, 6);
      if (hw == 3) poll(ws, mail + l * 8, ph, l < 16 || (l < 24 && !first) || (g4 == 2 && L == 0), tag);
      else if (L == 1) WAITV(0);
      else if (hw == 1) pull_unit(m);
      BAR();
      if (hw < 2 && L == 0) { dma3<0>(ws, hw * 21504, l16, wt + hw * 21504, 7); dma3<0>(ws, 43008 + hw * 21504, l16, wt + 43008 + hw * 21504, 7); }
      u32x4 xh[18], f2[9]; u32x2 st[2];
      {
        const u32 pm = first ? OOB : 0;
        const u32 sb = OFF_ST + 2 * L * ST_SZ + ((m - 1) * 8 * 128 + 112) * 8;
        st[0] = ld64(ws, pm + g4 * 2048 + t16 * 8, sb); st[1] = ld64(ws, pm + g4 * 2048 + t16 * 8, sb + 1024);
        const u32 xb = OFF_XB + 2 * L * XB_SZ + (m * 8 - 1) * 18432, xv = (first || t16 < 8) ? OOB : l16;
#pragma unroll
        for (int ks = 0; ks < 18; ++ks) xh[ks] = ldfrag(ws, xv, xb, (ks) * 1024);
      }
      if (L == 0) {
        WAITV(41);
        BAR();
        USE(xh[17]);
        WAITV(0);
        if (hw < 2) dma3<0>(ws, 86016 + hw * 21504, l16, wt + 86016 + hw * 21504, 7);
        unit_read(3, L_KS + min(hw, 2) * 9216, f2);
      } else if (hw == 2) l2pull(ws, l * 128, wt + WT_O + (m & 7) * 11520, 2);
      f32x4 acc[2] = {f32x4{0, 0, 0, 0}, f32x4{0, 0, 0, 0}};
      {
        int o1 = 64512; OPQ(o1);
        const LAS char* w0 = lds + l16 + hw * 2048; const LAS char* w1p = w0 + o1;
        const int d1 = hw == 3 ? 0 : 1024;
        __builtin_amdgcn_s_setprio(1);
        u32x4 a0[18], a1[18];
#pragma unroll
        for (int ks = 0; ks < 4; ++ks) { a0[ks] = lds128(w0 + ks * 7168); a1[ks] = lds128(w0 + ks * 7168 + d1); }
        SB();
#pragma unroll
        for (int ks = 0; ks < 18; ++ks) {
          u32x4 xv = xh[ks];
#pragma unroll
          for (int j = 0; j < 4; ++j) xv[j] = t16 == 0 ? ONES : xv[j];
          acc[0] = mfma16(a0[ks], xv, acc[0]);
          acc[1] = mfma16(a1[ks], xv, acc[1]);
          if (ks == 2 && L == 0) BAR();
          if (ks == 8 && L == 0) { WAITV(0); BAR(); }
          if (ks + 4 < 18) { const LAS char* p = ks + 4 < 9 ? w0 + (ks + 4) * 7168 : w1p + (ks - 5) * 7168; a0[ks + 4] = lds128(p); a1[ks + 4] = lds128(p + d1); }
          SB();
        }
        __builtin_amdgcn_s_setprio(0);
        if (t16 == 0) {
          *(LAS f32x4*)(lds + L_CSQ + hw * 128 + g4 * 16) = acc[0];
          if (hw != 3) *(LAS f32x4*)(lds + L_CSQ + hw * 128 + 64 + g4 * 16) = acc[1];
        }
      }
      float mean, rstd;
      stats16(st[0], st[1], mean, rstd);
      WAITL();
      BAR();
      if (hw != 2) {
        const f32x4 c0 = *(const LAS f32x4*)(lds + L_CSQ + hw * 128 + g4 * 16), c1 = *(const LAS f32x4*)(lds + L_CSQ + (hw == 3 ? 0 : hw * 128 + 64) + g4 * 16);
        const f32x4 v0 = (acc[0] - mean * c0) * rstd, v1 = (acc[1] - mean * c1) * rstd;
        if (hw < 2) *(LAS u32x4*)(lds + L_KS + hw * 9216 + g4 * 2304 + t16 * 16) = pk8(v0, v1);
        else if (g4 < 2) *(LAS u32x2*)(lds + L_RS + g4 * 1152 + t16 * 8) = u32x2{pk(v0[0], v0[1]), pk(v0[2], v0[3])};
      }
      if (hw >= 2) dma3<0>(ws, SLOT + (hw - 2) * 27648, l16, wt + WT_O + (hw - 2) * 27648, 9);
      if (L == 0 && un < 36) unit_store(3, un, f2);
      if (L == 0 && hw > 0 && un < 36) pull_unit(un);
      WAITL();
      BAR();
      attention(L);
      BAR();
      if (hw == 2) st64(tag, ws, g4 == 0 ? mail + s * 8 : (g4 == 2 && L == 0 ? mail + 256 + m * 8 : OOB), ph + 512);
      if (hw == 3) poll(ws, mail + l * 8, ph + 512, l < 16 || (g4 == 2 && L == 0), tag);
      else {
        if (hw < 2) dma3<0>(ws, SLOT + 55296 + hw * 18432, l16, wt + WT_O + 55296 + hw * 18432, 6);
        WAITV(0);
      }
      BAR();
      __builtin_amdgcn_s_sleep(20);
      if (hw == 2) l2pull(ws, l * 128, wt + WT_F + SLOT + (m & 7) * 13824, 2);
      if (hw == 3 && L == 0) pull_code();
      if (hw < 2) dma3<0>(ws, hw * 18432, l16, wt + WT_F + hw * 18432, 6);
      else if (L == 0) {
        const u32 cv = l < 36 ? ((hw - 2) * 36 + l) * 16 : OOB;
        u32 csb = OFF_CS + s * CS_PER; OPQ(csb);
        f32x4 sum = {0, 0, 0, 0};
        u32x4 part[18];
#pragma unroll
        for (int k = 0; k < 18; ++k) part[k] = ld128(ws, cv + (k % 3) * 1152, csb + (k / 3) * 3456);
        SB();
#pragma unroll
        for (int k = 0; k < 18; ++k) sum += __builtin_bit_cast(f32x4, part[k]);
        u32x4 o;
#pragma unroll
        for (int j = 0; j < 4; ++j) { const float h = hi16(pk(sum[j], sum[j])); o[j] = pk(h, sum[j] - h); }
        if (l < 36) *(LAS u32x4*)(lds + L_CS1 + cv) = o;
        WAITL();
      }
      BAR();
      if (hw == 2) st64(tag, ws, g4 == 0 ? mail + s * 8 : OOB, ph + 1024);
      if (hw < 2) dma3<0>(ws, SLOT + hw * 18432, l16, wt + WT_F + SLOT + hw * 18432, 6);
      if (hw == 3) poll(ws, mail + l * 8, ph + 1024, l < 16, tag);
      if (hw < 2) WAITV(0);
      BAR();
      if (hw < 2) {
        __builtin_amdgcn_s_sleep(24);
        dma3<1>(ws, 2 * SLOT + hw * 18432, l16, wt + WT_F + 2 * SLOT + hw * 18432, 6);
        WAITV(0);
        BAR();
        dma3<0>(ws, 3 * SLOT + hw * 18432, l16, wt + WT_F + 3 * SLOT + hw * 18432, 6);
        BAR();
        int sf = 0;
#pragma unroll 1
        for (int n = 1; n < 15; ++n) {
          dma3<2>(ws, sf + hw * 18432, l16, wt + WT_F + (n + 3) * SLOT + hw * 18432, 6);
          sf = sf == 3 * SLOT ? 0 : sf + SLOT;
          WAITV(18);
          BAR();
        }
        if (L == 0) { dma3<2>(ws, 2 * SLOT + hw * 18432, l16, wt + 2 * SLOT + hw * 18432, 6); WAITV(18); } else WAITV(0);
        BAR();
        if (L == 0) dma3<2>(ws, 3 * SLOT + hw * 9216, l16, wt + 3 * SLOT + hw * 9216, 3);
        BAR();
      } else {
#pragma unroll 1
        for (int n = 0; n < 18; ++n) {
          if (hw == 3 && (n == 7 || n == 9 || n == 12)) { const int k = n == 7 ? 0 : n == 9 ? 1 : 2; poll(ws, mail + l * 8, ph + 1280, (l >> 2) == k, tag); }
          if (hw == 3 && L == 0 && (n == 6 || n == 10)) poll(ws, mail + l * 8, 1792, g4 == (n == 10), tag);
          BAR();
          if (hw == 2) WAITV(0);
          if (hw == 2 && (n == 4 || n == 7 || n == 10)) st64(tag, ws, g4 == 0 && (t16 >> 2) == qd ? mail + (4 * ((n - 4) / 3) + r) * 8 : OOB, ph + 1280);
          if (hw == 2 && L == 0 && n >= 1 && n < 8) {
            const int uk = 16 * ((n - 1) >> 1) + m;
            if (n == 3 || n == 7) st64(tag, ws, g4 == 2 ? mail + ((n == 7 ? 16 : 0) + m) * 8 : OOB, 1792);
            if (uk < 36) {
              if (n & 1) unit_load(4, uk, L_OWN);
              else { unit_read(4, L_OWN, f2); unit_store(4, uk, f2); }
            }
          }
          if (hw == 2 && L == 0 && n == 13) l2pull(ws, l * 128, wt + (m & 7) * 16128, 2);
        }
      }
      BAR();
      if (hw == 2) st64(tag, ws, g4 == 0 && (t16 & 3) == r ? mail + qd * 8 : OOB, ph + 1536);
      if (hw == 3) poll(ws, mail + l * 8, ph + 1536, l < 4, tag);
      if (hw < 2 && L == 0) dma3<0>(ws, hw * 18432, l16, wt + hw * 18432, 6);
      BAR();
    }
    if (hw == 3) st64(tag, ws, l == 0 ? cu * 128 : OOB, OFF_NONCE);
  }
}
}

extern "C" void kernel_launch(void* const* d_in, const int* in_sizes, int n_in, void* d_out, int out_size, void* d_ws,
                              size_t ws_size, hipStream_t stream) {
  (void)in_sizes; (void)n_in; (void)out_size; (void)ws_size;
  (void)hipFuncSetAttribute((const void*)fused_fwd, hipFuncAttributeMaxDynamicSharedMemorySize, LDS_BYTES);
  fused_fwd<<<256, 512, LDS_BYTES, stream>>>((const u16*)d_in[0], (const u16*)d_in[1], (const u16*)d_in[2], (const u16*)d_in[4],
                                               (const u16*)d_in[5], (u16*)d_out, (char*)d_ws);
}
```
